```python
import torch

def _softmax(x, axis=-1):
    return torch.softmax(x, dim=axis)
def _gelu(x):
    return torch.nn.functional.gelu(x, approximate="tanh")
def _silu(x):
    return torch.nn.functional.silu(x)
def _sigmoid(x):
    return torch.sigmoid(x)
def _layernorm(x, axis=-1, eps=1e-5):
    m = x.mean(dim=axis, keepdim=True)
    v = x.var(dim=axis, keepdim=True, unbiased=False)
    return (x - m) / torch.sqrt(v + eps)
def _scatter_add(tgt, idx, src, axis=0):
    return tgt.clone().index_add_(axis, idx, src)
def _segment_sum(src, seg, n):
    out = torch.zeros((n,) + src.shape[1:], dtype=src.dtype)
    return out.index_add_(0, seg, src)
def _pad(x, pw):
    flat = tuple(v for pair in reversed(pw) for v in pair)
    return torch.nn.functional.pad(x, flat)

kernel_name = 'interleave_layers__sliding_block__dense_activation_gelu__prompt'

def setup_inputs():

    _w = (lambda a: (lambda t: t.to(torch.bfloat16) if t.is_floating_point() else t)(torch.as_tensor(a)))
    def _A_setup():
        B, S, D = 1, 2048, 512
        return {
            "x": _w((lambda *s: torch.randn(*s, dtype=torch.bfloat16) * 0.05)(B, S, D)),
            "wqkv": _w((lambda *s: torch.randn(*s, dtype=torch.bfloat16) * 0.05)(D, 3 * D)),
            "wo": _w((lambda *s: torch.randn(*s, dtype=torch.bfloat16) * 0.05)(D, D)),
        }
    _A_in = _A_setup()

    def _B_setup():
        B, S, D, F = 1, 2048, 512, 1792
        return {
            "x": _w((lambda *s: torch.randn(*s, dtype=torch.bfloat16) * 0.05)(B, S, D)),
            "w1": _w((lambda *s: torch.randn(*s, dtype=torch.bfloat16) * 0.8)(D, F) * (D ** -0.5)),
            "b1": _w((lambda *s: torch.randn(*s, dtype=torch.bfloat16) * 0.8)(F) * 0.01),
            "w2": _w((lambda *s: torch.randn(*s, dtype=torch.bfloat16) * 0.8)(F, D) * (F ** -0.5)),
            "b2": _w((lambda *s: torch.randn(*s, dtype=torch.bfloat16) * 0.8)(D) * 0.01),
        }
    _B_in = _B_setup()

    return {
        "A_x": _A_in["x"],
        "A_wqkv": _A_in["wqkv"],
        "A_wo": _A_in["wo"],
        "B_x": _B_in["x"],
        "B_w1": _B_in["w1"],
        "B_b1": _B_in["b1"],
        "B_w2": _B_in["w2"],
        "B_b2": _B_in["b2"],
    }

def reference(A_x, A_wqkv, A_wo, B_x, B_w1, B_b1, B_w2, B_b2):
    def _A_ref(x, wqkv, wo):
        B, S, D = x.shape
        H = 32; Dh = D // H; W = 256
        qkv = (lambda x, *d: x.permute(*d))((x @ wqkv).reshape(B, S, 3, H, Dh), 2, 0, 3, 1, 4)
        q, k, v = qkv[0], qkv[1], qkv[2]
        scores = torch.einsum("bhqd,bhkd->bhqk", q, k) * (Dh ** -0.5)
        idx = torch.arange(S)
        mask = (idx[:, None] - idx[None, :] >= 0) & (idx[:, None] - idx[None, :] < W)
        scores = torch.where(mask[None, None], scores, -1e9)
        p = _softmax(scores, axis=-1)
        o = torch.einsum("bhqk,bhkd->bhqd", p, v)
        return (lambda x, *d: x.permute(*d))(o, 0, 2, 1, 3).reshape(B, S, D) @ wo

    def _B_ref(x, w1, b1, w2, b2):
        h = torch.einsum("bsd,df->bsf", x, w1) + b1
        h = _gelu(h)
        return torch.einsum("bsf,fd->bsd", h, w2) + b2

    x = A_x
    x = x + _A_ref(_layernorm(x, axis=-1), A_wqkv, A_wo)
    x = x + _B_ref(_layernorm(x, axis=-1), B_w1, B_b1, B_w2, B_b2)
    x = x + _A_ref(_layernorm(x, axis=-1), A_wqkv, A_wo)
    x = x + _B_ref(_layernorm(x, axis=-1), B_w1, B_b1, B_w2, B_b2)
    return x
```

```cpp
#include <hip/hip_runtime.h>
#include <hip/hip_bf16.h>
typedef unsigned short u16;
__device__ __forceinline__ float b2f(u16 v){ return __uint_as_float(((unsigned)v)<<16); }
__device__ __forceinline__ u16 f2b(float f){ unsigned u=__float_as_uint(f); u += 0x7fffu + ((u>>16)&1u); return (u16)(u>>16); }
constexpr int S=2048,D=512,H=32,DH=16,W=256,F=1792;

__global__ void k_ln(const u16* x, u16* y){
  int r=blockIdx.x; int t=threadIdx.x;
  float v[8]; float s=0;
  for(int i=0;i<8;i++){ v[i]=b2f(x[r*D+t*8+i]); s+=v[i]; }
  for(int o=32;o;o>>=1) s+=__shfl_xor(s,o);
  float m=s/D; float q=0;
  for(int i=0;i<8;i++){ float d=v[i]-m; q+=d*d; }
  for(int o=32;o;o>>=1) q+=__shfl_xor(q,o);
  float rs=rsqrtf(q/D+1e-5f);
  for(int i=0;i<8;i++) y[r*D+t*8+i]=f2b((v[i]-m)*rs);
}
template<int MODE>
__global__ void k_gemm(const u16* A,const u16* B,u16* C,const u16* bias,const u16* res,int M,int N,int K){
  __shared__ float As[16][17], Bs[16][17];
  int tx=threadIdx.x&15, ty=threadIdx.x>>4;
  int row=blockIdx.y*16+ty, col=blockIdx.x*16+tx;
  float acc=0;
  for(int k0=0;k0<K;k0+=16){
    As[ty][tx]=b2f(A[row*K+k0+tx]);
    Bs[ty][tx]=b2f(B[(k0+ty)*N+col]);
    __syncthreads();
    for(int k=0;k<16;k++) acc+=As[ty][k]*Bs[k][tx];
    __syncthreads();
  }
  if(MODE==2||MODE==3) acc+=b2f(bias[col]);
  if(MODE==2){ float u=0.7978845608028654f*(acc+0.044715f*acc*acc*acc); acc=0.5f*acc*(1.f+tanhf(u)); }
  if(MODE==1||MODE==3) acc+=b2f(res[row*N+col]);
  C[row*N+col]=f2b(acc);
}
__global__ void k_attn(const u16* qkv,u16* o){
  int q=blockIdx.x*64+threadIdx.x; int h=blockIdx.y;
  float qv[16]; for(int d=0;d<16;d++) qv[d]=b2f(qkv[q*1536+h*16+d])*0.25f;
  float m=-1e30f,l=0,acc[16]; for(int d=0;d<16;d++) acc[d]=0;
  int k0=q-W+1; if(k0<0)k0=0;
  for(int k=k0;k<=q;k++){
    float s=0; for(int d=0;d<16;d++) s+=qv[d]*b2f(qkv[k*1536+512+h*16+d]);
    float mn=fmaxf(m,s); float a=__expf(m-mn), p=__expf(s-mn);
    l=l*a+p; for(int d=0;d<16;d++) acc[d]=acc[d]*a+p*b2f(qkv[k*1536+1024+h*16+d]);
    m=mn;
  }
  for(int d=0;d<16;d++) o[q*D+h*16+d]=f2b(acc[d]/l);
}
extern "C" void kernel_launch(void* const* d_in, const int* in_sizes, int n_in, void* d_out, int out_size, void* d_ws, size_t ws_size, hipStream_t stream){
  const u16* x=(const u16*)d_in[0]; const u16* wqkv=(const u16*)d_in[1]; const u16* wo=(const u16*)d_in[2];
  const u16* w1=(const u16*)d_in[4]; const u16* b1=(const u16*)d_in[5]; const u16* w2=(const u16*)d_in[6]; const u16* b2=(const u16*)d_in[7];
  u16* ws=(u16*)d_ws;
  u16* xn=ws; u16* qkv=xn+S*D; u16* o=qkv+S*3*D; u16* x1=o+S*D; u16* h=x1+S*D; u16* x2=h+S*F;
  const u16* cur=x;
  for(int L=0;L<2;L++){
    k_ln<<<S,64,0,stream>>>(cur,xn);
    k_gemm<0><<<dim3(1536/16,S/16),256,0,stream>>>(xn,wqkv,qkv,nullptr,nullptr,S,1536,D);
    k_attn<<<dim3(S/64,H),64,0,stream>>>(qkv,o);
    k_gemm<1><<<dim3(D/16,S/16),256,0,stream>>>(o,wo,x1,nullptr,cur,S,D,D);
    k_ln<<<S,64,0,stream>>>(x1,xn);
    k_gemm<2><<<dim3(F/16,S/16),256,0,stream>>>(xn,w1,h,b1,nullptr,S,F,D);
    u16* dst = L==0? x2 : (u16*)d_out;
    k_gemm<3><<<dim3(D/16,S/16),256,0,stream>>>(h,w2,dst,b2,x1,S,D,F);
    cur=x2;
  }
}
```

```cpp
#include <hip/hip_runtime.h>
#include <type_traits>
#include <utility>

#define STAMPS 0

#define GAS __attribute__((address_space(1)))
#define LAS __attribute__((address_space(3)))
#define DI __device__ __forceinline__
typedef unsigned u32;
typedef unsigned long long u64;
typedef u32 v4u __attribute__((ext_vector_type(4)));
typedef u32 v2u __attribute__((ext_vector_type(2)));
typedef int v4i __attribute__((ext_vector_type(4)));
typedef short s4 __attribute__((ext_vector_type(4)));
typedef __bf16 bf8 __attribute__((ext_vector_type(8)));
typedef __bf16 bf2 __attribute__((ext_vector_type(2)));
typedef float f2 __attribute__((ext_vector_type(2)));
typedef float f4 __attribute__((ext_vector_type(4)));
typedef float f16v __attribute__((ext_vector_type(16)));

template <class F, int... I> DI void sfor_impl(F&& f, std::integer_sequence<int, I...>) { (f(std::integral_constant<int, I>{}), ...); }
template <int N, class F> DI void sfor(F&& f) { sfor_impl(f, std::make_integer_sequence<int, N>{}); }

constexpr int KB = 1024, MB = 1024 * 1024;
constexpr int WS_EPOCH = 0;
constexpr int WS_FLAG = 8 * KB;
constexpr int WS_STAMP = 512 * KB;
constexpr int WS_XF = 1 * MB;
constexpr int WS_ST = WS_XF + 6 * MB;
constexpr int WS_KV = WS_ST + 1 * MB;
constexpr int WS_OF = WS_KV + 8 * MB;
constexpr int WS_HF = WS_OF + 4 * MB;
constexpr int WS_END = WS_HF + 14 * MB;
constexpr int L_KV = 0, L_QB = 48 * KB, L_ONES = 52 * KB, L_WQ = 60 * KB, L_WO = 92 * KB, L_W1 = 0, L_X2 = 0, L_X4 = 40 * KB;
constexpr int L_AC = 156 * KB, L_CSQ = L_AC + 1024, L_CS1 = L_CSQ + 384, L_B1 = L_CS1 + 512, LDS_BYTES = 160 * KB;

struct Params { const void *x, *wqkv, *wo, *w1, *b1, *w2, *b2; void* out; unsigned char* ws; };

DI v4i mk_rsrc(const void* p, u32 bytes) { v4i r; r.x = (int)(u32)(u64)p; r.y = (int)((u32)((u64)p >> 32) & 0xffffu); r.z = (int)bytes; r.w = 0x00020000; return r; }
template <int OFF, bool SC1> DI void bld16(v4u& d, u32 voff, v4i rs, u32 soff) {
  if constexpr (SC1) asm volatile("buffer_load_dwordx4 %0, %1, %2, %3 offen offset:%4 sc1" : "=v"(d) : "v"(voff), "s"(rs), "s"(soff), "i"(OFF));
  else asm volatile("buffer_load_dwordx4 %0, %1, %2, %3 offen offset:%4" : "=v"(d) : "v"(voff), "s"(rs), "s"(soff), "i"(OFF));
}
template <int OFF> DI void bst16_sc1(v4u d, u32 voff, v4i rs, u32 soff) {
  asm volatile("buffer_store_dwordx4 %0, %1, %2, %3 offen offset:%4 sc1\n\ts_nop 1" ::"v"(d), "v"(voff), "s"(rs), "s"(soff), "i"(OFF) : "memory");
}
DI void bst8_sc1(v2u d, u32 voff, v4i rs, u32 soff) { asm volatile("buffer_store_dwordx2 %0, %1, %2, %3 offen sc1" ::"v"(d), "v"(voff), "s"(rs), "s"(soff) : "memory"); }
template <bool SC1 = false> DI void dma16(u32 lds, u32 voff, v4i rs, u32 soff) {
  if constexpr (SC1) asm volatile("s_mov_b32 m0, %0\n\ts_nop 0\n\tbuffer_load_dwordx4 %1, %2, %3 offen sc1 lds" ::"s"(lds), "v"(voff), "s"(rs), "s"(soff) : "memory");
  else asm volatile("s_mov_b32 m0, %0\n\ts_nop 0\n\tbuffer_load_dwordx4 %1, %2, %3 offen lds" ::"s"(lds), "v"(voff), "s"(rs), "s"(soff) : "memory");
}
template <int N> DI void wait_vm(v4u& x) { asm volatile("s_waitcnt vmcnt(%1)" : "+v"(x) : "i"(N)); }
#define WAIT_VM0() asm volatile("s_waitcnt vmcnt(0)" ::: "memory")
#define BARRIER() asm volatile("s_waitcnt lgkmcnt(0)\n\ts_barrier" ::: "memory")
#define SBAR() __builtin_amdgcn_sched_barrier(0)
DI f16v mfma(v4u a, v4u b, f16v c) { return __builtin_amdgcn_mfma_f32_32x32x16_bf16(__builtin_bit_cast(bf8, a), __builtin_bit_cast(bf8, b), c, 0, 0, 0); }
DI u32 pk(float lo, float hi) { bf2 r = __builtin_convertvector((f2){lo, hi}, bf2); return __builtin_bit_cast(u32, r); }
DI float blo(u32 v) { return __builtin_bit_cast(float, v << 16); }
DI float bhi(u32 v) { return __builtin_bit_cast(float, v & 0xffff0000u); }
template <class T> DI T ldsr(u32 a) { return *(LAS T*)(size_t)a; }
template <class T> DI void ldsw(u32 a, T v) { *(LAS T*)(size_t)a = v; }
DI v4u trfrag(u32 a) {
  const v2u lo = __builtin_bit_cast(v2u, __builtin_amdgcn_ds_read_tr16_b64_v4i16((LAS s4*)(size_t)a));
  const v2u hi = __builtin_bit_cast(v2u, __builtin_amdgcn_ds_read_tr16_b64_v4i16((LAS s4*)(size_t)(a + 512)));
  return (v4u){lo.x, lo.y, hi.x, hi.y};
}
DI float xhalf(float v) {
  auto r = __builtin_amdgcn_permlane32_swap(__builtin_bit_cast(u32, v), __builtin_bit_cast(u32, v), false, false);
  return __builtin_bit_cast(float, (threadIdx.x & 32) ? r[0] : r[1]);
}
DI float mx3(float a, float b, float c) { return __builtin_fmaxf(__builtin_fmaxf(a, b), c); }
DI float max16(const f16v& s, float m) {
  float a = mx3(m, s[0], s[1]), b = mx3(s[2], s[3], s[4]);
  a = mx3(a, s[5], s[6]); b = mx3(b, s[7], s[8]); a = mx3(a, s[9], s[10]); b = mx3(b, s[11], s[12]); a = mx3(a, s[13], s[14]);
  return mx3(a, b, s[15]);
}
DI v4u pk8(const f16v& v, int o) { return (v4u){pk(v[o], v[o + 1]), pk(v[o + 2], v[o + 3]), pk(v[o + 4], v[o + 5]), pk(v[o + 6], v[o + 7])}; }
constexpr u32 SPIN_CAP = 1u << 20;
DI u32 fresh(u32 v) { asm volatile("" : "+v"(v)); return v; }
DI u32 sfresh(u32 v) { asm volatile("" : "+s"(v)); return v; }

template <int NT, int KS, int WIN, bool SWAPLAST, int S0, int S1, class FI, class FA>
DI void gemm_steps(f16v (&acc)[NT], v4u (&win)[WIN], FI&& issue, FA&& addr) {
  constexpr int DEP = 4, N0 = S0 * NT, N1 = S1 * NT;
  v4u ring[DEP + 1];
  sfor<DEP>([&](auto i) { ring[(N0 + i) % (DEP + 1)] = trfrag(addr(std::integral_constant<int, N0 + i>{})); });
  SBAR();
  sfor<N1 - N0>([&](auto ic) {
    constexpr int i = N0 + ic, s = i / NT, t = i % NT;
    if constexpr (t == 0) wait_vm<(KS - 1 - s < WIN - 1 ? KS - 1 - s : WIN - 1)>(win[s % WIN]);
    if constexpr (i + DEP < N1) ring[(i + DEP) % (DEP + 1)] = trfrag(addr(std::integral_constant<int, i + DEP>{}));
    if constexpr (SWAPLAST && t == NT - 1) acc[t] = mfma(win[s % WIN], ring[i % (DEP + 1)], acc[t]);
    else acc[t] = mfma(ring[i % (DEP + 1)], win[s % WIN], acc[t]);
    if constexpr (t == NT - 1 && s + WIN < KS) { SBAR(); issue(std::integral_constant<int, s + WIN>{}, win[s % WIN]); }
    SBAR();
  });
}

struct Ctx {
  unsigned char* ws; v4i rws; int grp, m, cu, w;
};
#define STAMP() do {} while (0)
DI f2 ln_ac(float s1, float s2) {
  const float mean = s1 * (1.0f / 512.0f), var = __builtin_fmaxf(s2 * (1.0f / 512.0f) - mean * mean, 0.0f);
  const float a = __builtin_amdgcn_rsqf(var + 1e-5f); return (f2){a, -mean * a};
}
DI u32 trbase(u32 lane) { return (4 * (lane >> 5) + ((lane & 15) >> 2)) * 64 + ((lane >> 4) & 1) * 32 + (lane & 3) * 8; }

template <int HD> struct Attn {
  v4u qf; f16v o; float mrun; u32 ka, va, vstr; int dq;
  DI void init(u32 lane, v4u q) {
    qf = q; o = (f16v){}; mrun = -__builtin_inff();
    const bool real = (lane & 16) == 0;
    ka = L_KV + HD * 1024 + lane * 16; va = real ? L_KV + 2048 + (lane + 16 * HD) * 16 : L_ONES + lane * 16; vstr = real ? 4096u : 0u;
    dq = (int)(lane & 31) - 4 * (int)(lane >> 5);
  }
  DI void pv(const f16v& pe, int kb) { const u32 a = va + kb * vstr; o = mfma(ldsr<v4u>(a), pk8(pe, 0), o); o = mfma(ldsr<v4u>(a + 1024), pk8(pe, 8), o); }
  template <bool DIAG> DI void segment(int kbm, int k0, int k1) {
    const f16v zero = {};
    f16v sm = zero; float mx = mrun;
    if (kbm >= 0) {
      sm = mfma(ldsr<v4u>(ka + kbm * 4096), qf, zero);
      const int dqf = (int)fresh((u32)dq);
      sfor<16>([&](auto r) { constexpr int c = (r & 3) + 8 * (r >> 2); if ((c <= dqf) != DIAG) sm[(int)r] = -__builtin_inff(); });
      mx = max16(sm, mx);
    }
    for (int kb = k0; kb < k1; ++kb) mx = max16(mfma(ldsr<v4u>(ka + kb * 4096), qf, zero), mx);
    mx = __builtin_fmaxf(mx, xhalf(mx));
    if constexpr (!DIAG) { const float al = __builtin_amdgcn_exp2f(mrun - mx); sfor<9>([&](auto r) { o[(int)r] *= al; }); }
    mrun = mx;
    if (kbm >= 0) { sfor<16>([&](auto r) { sm[(int)r] = __builtin_amdgcn_exp2f(sm[(int)r] - mx); }); pv(sm, kbm); }
    f16v nm; sfor<16>([&](auto r) { nm[(int)r] = -mx; });
    for (int kb = k0; kb < k1; ++kb) {
      f16v s = mfma(ldsr<v4u>(ka + kb * 4096), qf, nm);
      sfor<16>([&](auto r) { s[(int)r] = __builtin_amdgcn_exp2f(s[(int)r]); });
      pv(s, kb);
    }
  }
  DI v4u finish() { const float rl = __builtin_amdgcn_rcpf(o[8]); sfor<8>([&](auto r) { o[(int)r] *= rl; }); return pk8(o, 0); }
};

__global__ void __launch_bounds__(512, 2) fwd(Params p) {
  extern __shared__ __attribute__((aligned(16))) unsigned char smem[];
  const int w0 = __builtin_amdgcn_readfirstlane(threadIdx.x >> 6);
#define IDS() const int w = sfresh(w0), bid = sfresh(blockIdx.x), grp = (bid & 7) * 2 + (bid >> 7), m = (bid >> 3) & 15, cu = grp * 16 + m, hw = w & 3, cb = m & 7, th = m >> 3, \
              rt = w & 1, kh = (w >> 1) & 1, T13 = 4 * grp + hw, T24 = 4 * grp + 2 * th + rt; (void)cu; (void)cb; (void)kh; (void)T13; (void)T24
  IDS();
  const v4i rws = mk_rsrc(p.ws, WS_END);
  const u32 lane0 = threadIdx.x & 63;
  STAMP();
  auto halo_load = [&](int L, u32 l16) {
    IDS();
    if (w >= (grp > 1 ? 0 : 4)) { const u32 so = WS_KV + L * 4 * MB + ((4 * grp - 8 + w) * 16 + m) * 4096;
                                  sfor<4>([&](auto i) { dma16<true>(L_KV + w * 4096 + i * 1024, l16, rws, so + i * 1024); }); }
  };
  if (w >= 4) {
    const v4i rq = mk_rsrc(p.wqkv, 1536 * KB), ro = mk_rsrc(p.wo, 512 * KB), r1 = mk_rsrc(p.w1, 1792 * KB), r2 = mk_rsrc(p.w2, 1792 * KB);
    const u32 wrow = lane0 >> 2, wch = (lane0 & 3) * 16;
    auto stage_wq = [&](int s0, int s1, bool nat) {
      IDS();
      const u32 vo = (nat ? 16 * ((wrow >> 2) & 1) + 4 * (wrow >> 3) + (wrow & 3) : wrow) * 3072 + wch + m * 64;
      for (int s = s0 + hw; s < s1; s += 4) {
        const u32 so = (nat ? 32 * (s >> 1) + 8 * (s & 1) : 16 * s) * 3072;
        dma16(L_WQ + s * 3072, vo, rq, so); dma16(L_WQ + s * 3072 + 1024, vo, rq, so + 1024); dma16(L_WQ + s * 3072 + 2048, vo, rq, so + 2048);
      }
    };
    const u32 vo24 = wrow * 1024 + wch + cb * 128, vo1 = wrow * 3584 + wch + m * 256;
    auto stage_wo = [&]() { IDS(); for (int s = hw; s < 32; s += 4) { dma16(L_WO + s * 2048, vo24, ro, s * 16384); dma16(L_WO + s * 2048 + 1024, vo24, ro, s * 16384 + 64); } };
    auto stage_w1 = [&](int s0, int s1) {
      IDS();
      if (m >= 14) return;
      for (int s = s0 + hw; s < s1; s += 4) {
        const u32 so = s * 16 * 3584;
        dma16(L_W1 + s * 4096, vo1, r1, so); dma16(L_W1 + s * 4096 + 1024, vo1, r1, so + 64); dma16(L_W1 + s * 4096 + 2048, vo1, r1, so + 128); dma16(L_W1 + s * 4096 + 3072, vo1, r1, so + 192);
      }
    };
    auto stage_w2 = [&](int i0, int i1, int p0) {
      IDS();
      for (int i = i0 + hw; i < i1; i += 4) {
        const u32 la = (i + p0) * 4096, so = i * 16384;
        dma16(la, vo24, r2, so); dma16(la + 1024, vo24, r2, so + 64); dma16(la + 2048, vo24, r2, so + 56 * 16384); dma16(la + 3072, vo24, r2, so + 56 * 16384 + 64);
      }
    };
    stage_wq(0, 32, true);
    if (w == 4 && m < 14) { const u32 v = ((const GAS u32*)p.b1)[m * 64 + lane0]; ldsw<f2>(L_B1 + lane0 * 8, (f2){blo(v), bhi(v)}); }
    const v4u ones = {0x3f803f80u, 0x3f803f80u, 0x3f803f80u, 0x3f803f80u};
    for (int L = 0; L < 2; ++L) {
      IDS();
      const u32 lane = fresh(lane0), l16 = lane * 16;
      auto gather_stats = [&](int buf) {
        v4u q[4]; const u32 so = WS_ST + buf * 256 * KB + T13 * 4096;
        bld16<0, true>(q[0], l16, rws, so); bld16<1024, true>(q[1], l16, rws, so); bld16<2048, true>(q[2], l16, rws, so); bld16<3072, true>(q[3], l16, rws, so);
        asm volatile("s_waitcnt vmcnt(0)" : "+v"(q[0]), "+v"(q[1]), "+v"(q[2]), "+v"(q[3]));
        f4 s = (__builtin_bit_cast(f4, q[0]) + __builtin_bit_cast(f4, q[1])) + (__builtin_bit_cast(f4, q[2]) + __builtin_bit_cast(f4, q[3]));
        sfor<4>([&](auto i) { s[(int)i] += __shfl_xor(s[(int)i], 16); s[(int)i] += xhalf(s[(int)i]); });
        const f2 a = ln_ac(s[0], s[1]), b = ln_ac(s[2], s[3]);
        if (lane < 16) ldsw<f4>(L_AC + hw * 256 + l16, (f4){a.x, a.y, b.x, b.y});
      };
      auto colsum = [&](u32 base, int stride, u32 dst) {
        f16v a = {}; const u32 b0 = base + trbase(lane);
        for (int s = 0; s < 32; s += 4) sfor<4>([&](auto i) { a = mfma(trfrag(b0 + (s + i) * stride), ones, a); });
        if ((lane & 31) == 0) sfor<4>([&](auto g) { ldsw<f4>(dst + (8 * g + 4 * (lane >> 5)) * 4, (f4){a[4 * g], a[4 * g + 1], a[4 * g + 2], a[4 * g + 3]}); });
      };
      ldsw<v4u>(L_ONES + (hw & 1) * 1024 + l16, ones);
      if (L == 0) {
        WAIT_VM0(); BARRIER();
        STAMP();
        if (hw < 3) colsum(L_WQ + hw * 1024, 3072, L_CSQ + hw * 128);
      } else { STAMP(); gather_stats(1); }
      BARRIER();
      STAMP();
      stage_wo(); stage_w1(15, 23);
      BARRIER();
      STAMP();
      {
        const int qb = 3 - hw;
        Attn<1> at; at.init(lane, ldsr<v4u>(L_QB + qb * 1024 + l16));
        at.segment<true>(8 + qb, 8, 8 + qb);
        STAMP();
        if (grp > 0) {
          BARRIER();
          STAMP();
          halo_load(L, l16);
          WAIT_VM0(); BARRIER();
          STAMP();
          at.segment<false>(grp > 1 ? qb : -1, grp > 1 ? qb + 1 : 4, 8);
        } else { STAMP(); STAMP(); }
        bst16_sc1<0>(at.finish(), l16, rws, WS_OF + L * 2 * MB + ((4 * grp + qb) * 32 + 2 * m + 1) * 1024);
      }
      STAMP();
      WAIT_VM0(); BARRIER();
      STAMP();
      stage_w1(4, 15);
      BARRIER();
      STAMP();
      BARRIER();
      STAMP();
      stage_w1(23, 32);
      BARRIER();
      STAMP();
      stage_w1(0, 4); WAIT_VM0();
      BARRIER();
      STAMP();
      if (m < 14) gather_stats(2 * L);
      stage_w2(0, 7, 32);
      if (L == 0 && m < 14) colsum(L_W1 + hw * 1024, 4096, L_CS1 + hw * 128);
      BARRIER();
      STAMP();
      stage_w2(7, 39, -7);
      STAMP();
      BARRIER();
      STAMP();
      WAIT_VM0();
      BARRIER();
      STAMP();
      BARRIER();
      stage_w2(39, 46, -7); stage_w2(46, 56, -46); WAIT_VM0();
      BARRIER();
      if (L == 0) stage_wq(0, 22, false);
      BARRIER();
      STAMP();
      if (L == 0) {
        stage_wq(22, 32, false);
        BARRIER();
        STAMP();
        WAIT_VM0();
        BARRIER();
      }
    }
    STAMP();
    return;
  }
  u64 tag = 0;
  if (w == 0) { const int g = grp + (lane0 < 3 ? lane0 : 0); tag = __hip_atomic_load((GAS u64*)(p.ws + WS_EPOCH + (g < 16 ? g : grp) * 256), __ATOMIC_RELAXED, __HIP_MEMORY_SCOPE_AGENT) + 1; }
  const u64 tag0 = ((u64)(u32)__builtin_amdgcn_readfirstlane((u32)(tag >> 32)) << 32) | (u32)__builtin_amdgcn_readfirstlane((u32)tag);
  v4u res[2], b2p[2];
  {
    const GAS unsigned char* xr = (const GAS unsigned char*)p.x + (T24 * 32 + (lane0 & 31)) * 1024 + (cb * 64 + kh * 32 + 4 * (lane0 >> 5)) * 2;
    const GAS unsigned char* br = (const GAS unsigned char*)p.b2 + (cb * 64 + kh * 32 + 4 * (lane0 >> 5)) * 2;
    sfor<2>([&](auto f) { const v2u a = *(const GAS v2u*)(xr + f * 32), b = *(const GAS v2u*)(xr + f * 32 + 16); res[f] = (v4u){a.x, a.y, b.x, b.y};
                          const v2u c = *(const GAS v2u*)(br + f * 32), d = *(const GAS v2u*)(br + f * 32 + 16); b2p[f] = (v4u){c.x, c.y, d.x, d.y}; });
    asm volatile("" : "+v"(res[0]), "+v"(res[1]), "+v"(b2p[0]), "+v"(b2p[1]));
  }
  for (int L = 0; L < 2; ++L) {
    IDS();
    const u32 lane = fresh(lane0), l16 = lane * 16, h = lane >> 5, t32 = lane & 31;
    auto poll = [&](int sid, u32 n0, u32 n1) {
      GAS u64* f = (GAS u64*)(p.ws + WS_FLAG + (sid * 256 + cu) * 128) + (lane & 15);
      const bool act = lane >= n0 && lane < n1;
      for (u32 spins = 0; spins < SPIN_CAP; ++spins) {
        u64 v = tag0; if (act) v = __hip_atomic_load(f, __ATOMIC_RELAXED, __HIP_MEMORY_SCOPE_AGENT);
        if (__all(v == tag0)) break;
        __builtin_amdgcn_s_sleep(1);
      }
    };
    auto sync = [&](int sid) {
      if (w == 0) { if (lane < 16) __hip_atomic_store((GAS u64*)(p.ws + WS_FLAG + (sid * 256 + grp * 16 + lane) * 128) + m, tag0, __ATOMIC_RELAXED, __HIP_MEMORY_SCOPE_AGENT);
                    poll(sid, 0, 16); }
    };
    auto res_epilogue = [&](f16v (&acc)[2], u32 xch, bool bias, int buf, bool last) {
      f16v v; const u32 xa = fresh(xch + (w ^ 2) * 4096 + l16);
      sfor<4>([&](auto g) { const f4 o = ldsr<f4>(xa + g * 1024);
                            sfor<4>([&](auto i) { v[4 * g + i] = (kh ? acc[1][4 * g + i] : acc[0][4 * g + i]) + o[(int)i]; }); });
      sfor<8>([&](auto i) { const u32 r = res[i / 4][i % 4], b = b2p[i / 4][i % 4];
                            v[2 * i] += blo(r) + (bias ? blo(b) : 0.0f); v[2 * i + 1] += bhi(r) + (bias ? bhi(b) : 0.0f); });
      res[0] = pk8(v, 0); res[1] = pk8(v, 8);
      if (!last) {
        const u32 so = WS_XF + buf * 2 * MB + (T24 * 32 + cb * 4 + kh * 2) * 1024;
        bst16_sc1<0>(res[0], l16, rws, so); bst16_sc1<1024>(res[1], l16, rws, so);
        float s1 = 0.f, s2 = 0.f;
        sfor<8>([&](auto i) { const float a = blo(res[i / 4][i % 4]), b = bhi(res[i / 4][i % 4]); s1 += a + b; s2 = __builtin_fmaf(a, a, s2); s2 = __builtin_fmaf(b, b, s2); });
        s1 += xhalf(s1); s2 += xhalf(s2);
        if (lane < 32) bst8_sc1((v2u){__builtin_bit_cast(u32, s1), __builtin_bit_cast(u32, s2)}, lane * 8, rws, WS_ST + buf * 256 * KB + (T24 * 16 + cb * 2 + kh) * 256);
      } else {
        GAS unsigned char* o = (GAS unsigned char*)p.out + (T24 * 32 + t32) * 1024 + (cb * 64 + kh * 32 + 8 * h) * 2;
        sfor<2>([&](auto f) {
          auto a = __builtin_amdgcn_permlane32_swap(res[f].x, res[f].z, false, false), b = __builtin_amdgcn_permlane32_swap(res[f].y, res[f].w, false, false);
          *(GAS v4u*)(o + f * 32) = (v4u){a[0], b[0], a[1], b[1]}; });
      }
    };
    auto xch_write = [&](f16v (&acc)[2], u32 xch) {
      const u32 xa = fresh(xch + w * 4096 + l16);
      sfor<4>([&](auto g) { ldsw<f4>(xa + g * 1024, kh ? (f4){acc[0][4 * g], acc[0][4 * g + 1], acc[0][4 * g + 2], acc[0][4 * g + 3]}
                                                       : (f4){acc[1][4 * g], acc[1][4 * g + 1], acc[1][4 * g + 2], acc[1][4 * g + 3]}); });
    };
    v4u qf;
    {
      f16v acc1[3] = {};
      const u32 wb0 = fresh(L_WQ + trbase(lane)), wb1 = fresh(wb0 + 48 * KB);
      auto addr = [&](auto i) { constexpr int o = i * 1024; return o < 48 * KB ? wb0 + o : wb1 + (o - 48 * KB); };
      if (L == 0) {
        const v4i rx = mk_rsrc(p.x, 2 * MB);
        v4u xa[32];
        const u32 vo = (lane & 15) * 1024 + (lane >> 4) * 16, so = T13 * 32 * 1024;
        sfor<16>([&](auto u) { bld16<u * 64, false>(xa[2 * u], vo, rx, so); bld16<u * 64, false>(xa[2 * u + 1], vo, rx, so + 16384); });
        BARRIER();
        STAMP();
        float s1 = 0.f, s2 = 0.f;
        v4u ring[5];
        sfor<4>([&](auto i) { ring[i] = trfrag(addr(i)); });
        SBAR();
        sfor<96>([&](auto ic) {
          constexpr int i = ic, s = i / 3, t = i % 3;
          if constexpr (t == 0 && (s & 1) == 0) {
            asm volatile("s_waitcnt vmcnt(%2)" : "+v"(xa[s]), "+v"(xa[s + 1]) : "i"(30 - s));
            sfor<4>([&](auto d) { auto r = __builtin_amdgcn_permlane16_swap(xa[s][(int)d], xa[s + 1][(int)d], false, false); xa[s][(int)d] = r[0]; xa[s + 1][(int)d] = r[1]; });
          }
          if constexpr (i + 4 < 96) ring[(i + 4) % 5] = trfrag(addr(std::integral_constant<int, i + 4>{}));
          if constexpr (t == 2) acc1[t] = mfma(xa[s], ring[i % 5], acc1[t]); else acc1[t] = mfma(ring[i % 5], xa[s], acc1[t]);
          if constexpr (t < 2) sfor<2>([&](auto d) { const u32 eu = xa[s][2 * t + d]; const bf2 e = __builtin_bit_cast(bf2, eu);
                                                     s1 = __builtin_amdgcn_fdot2_f32_bf16(e, __builtin_bit_cast(bf2, 0x3f803f80u), s1, false); s2 = __builtin_amdgcn_fdot2_f32_bf16(e, e, s2, false); });
          SBAR();
        });
        s1 += xhalf(s1); s2 += xhalf(s2);
        if (lane < 32) ldsw<f2>(L_AC + hw * 256 + lane * 8, ln_ac(s1, s2));
      } else {
        constexpr int WIN = 12;
        v4u win[WIN];
        STAMP();
        const u32 so = WS_XF + 1 * 2 * MB + T13 * 32 * 1024;
        auto issue = [&](auto i, v4u& d) { bld16<(i & 3) * 1024, true>(d, l16, rws, so + (i >> 2) * 4096); };
        sfor<WIN>([&](auto i) { issue(i, win[i]); });
        gemm_steps<3, 32, WIN, true, 0, 32>(acc1, win, issue, addr);
      }
      BARRIER();
      STAMP();
      const u32 la = fresh(L_AC + hw * 256), lc = fresh(L_CSQ + 16 * h);
      const f2 ac = ldsr<f2>(la + t32 * 8);
      constexpr float QS = 0.25f * 1.4426950408889634f;
      const u32 so = WS_KV + L * 4 * MB + (T13 * 16 + m) * 4096, lk = fresh(L_KV + (8 + hw) * 4096 + l16);
      f16v v;
      sfor<4>([&](auto g) { const f4 cs = ldsr<f4>(lc + 128 + 32 * g); sfor<4>([&](auto i) { v[4 * g + i] = __builtin_fmaf(ac.x, acc1[1][4 * g + i], ac.y * cs[(int)i]); }); });
      { const v4u k0 = pk8(v, 0), k1 = pk8(v, 8); bst16_sc1<0>(k0, l16, rws, so); bst16_sc1<1024>(k1, l16, rws, so); ldsw<v4u>(lk, k0); ldsw<v4u>(lk + 1024, k1); }
      const float csv = ldsr<float>(L_CSQ + 256 + t32 * 4);
      const u32 lt = la + 32 * h;
      sfor<4>([&](auto g) { const f4 a = ldsr<f4>(lt + 64 * g), b = ldsr<f4>(lt + 64 * g + 16);
                            v[4 * g] = __builtin_fmaf(a[0], acc1[2][4 * g], a[1] * csv); v[4 * g + 1] = __builtin_fmaf(a[2], acc1[2][4 * g + 1], a[3] * csv);
                            v[4 * g + 2] = __builtin_fmaf(b[0], acc1[2][4 * g + 2], b[1] * csv); v[4 * g + 3] = __builtin_fmaf(b[2], acc1[2][4 * g + 3], b[3] * csv); });
      { const v4u v0 = pk8(v, 0), v1 = pk8(v, 8); bst16_sc1<2048>(v0, l16, rws, so); bst16_sc1<3072>(v1, l16, rws, so); ldsw<v4u>(lk + 2048, v0); ldsw<v4u>(lk + 3072, v1); }
      const float aq = ac.x * QS, cq = ac.y * QS;
      sfor<4>([&](auto g) { const f4 cs = ldsr<f4>(lc + 32 * g); sfor<4>([&](auto i) { v[4 * g + i] = __builtin_fmaf(aq, acc1[0][4 * g + i], cq * cs[(int)i]); }); });
      qf = pk8(v, 0); ldsw<v4u>(L_QB + hw * 1024 + l16, pk8(v, 8));
      WAIT_VM0();
    }
    BARRIER();
    STAMP();
    if (w == 0 && (lane == 1 || lane == 2) && grp + lane < 16)
      __hip_atomic_store((GAS u64*)(p.ws + WS_FLAG + ((L * 5 + 0) * 256 + cu + 16 * lane) * 128) + lane, tag, __ATOMIC_RELAXED, __HIP_MEMORY_SCOPE_AGENT);
    {
      const int qb = hw;
      Attn<0> at; at.init(lane, qf);
      at.segment<true>(8 + qb, 8, 8 + qb);
      STAMP();
      if (grp > 0) {
        if (w == 0) poll(L * 5 + 0, 1, grp > 1 ? 3 : 2);
        BARRIER();
        STAMP();
        halo_load(L, l16);
        WAIT_VM0(); BARRIER();
        STAMP();
        at.segment<false>(grp > 1 ? qb : -1, grp > 1 ? qb + 1 : 4, 8);
      } else { STAMP(); STAMP(); }
      bst16_sc1<0>(at.finish(), l16, rws, WS_OF + L * 2 * MB + ((4 * grp + qb) * 32 + 2 * m) * 1024);
    }
    STAMP();
    WAIT_VM0(); BARRIER();
    STAMP();
    sync(L * 5 + 1);
    BARRIER();
    STAMP();
    {
      constexpr int WIN = 16;
      v4u win[WIN];
      f16v acc2[2] = {};
      const u32 so = WS_OF + L * 2 * MB + (T24 * 32 + kh * 16) * 1024, wb = fresh(L_WO + kh * 32 * KB + trbase(lane));
      auto issue = [&](auto i, v4u& d) { bld16<(i & 3) * 1024, true>(d, l16, rws, so + (i >> 2) * 4096); };
      auto addr = [&](auto i) { return wb + i * 1024; };
      sfor<WIN>([&](auto i) { issue(i, win[i]); });
      gemm_steps<2, 16, WIN, false, 0, 16>(acc2, win, issue, addr);
      xch_write(acc2, L_X2);
      BARRIER();
      STAMP();
      res_epilogue(acc2, L_X2, false, 2 * L, false);
    }
    WAIT_VM0(); BARRIER();
    STAMP();
    sync(L * 5 + 2);
    BARRIER();
    STAMP();
    if (m < 14) {
      constexpr int WIN = 12;
      v4u win[WIN];
      f16v acc3[4] = {};
      const u32 so = WS_XF + 2 * L * 2 * MB + T13 * 32 * 1024, wb0 = fresh(L_W1 + trbase(lane)), wb1 = fresh(wb0 + 63 * KB), wb2 = fresh(wb0 + 126 * KB);
      auto issue = [&](auto i, v4u& d) { bld16<(i & 3) * 1024, true>(d, l16, rws, so + (i >> 2) * 4096); };
      auto addr = [&](auto i) { constexpr int o = i * 1024; return o < 63 * KB ? wb0 + o : o < 126 * KB ? wb1 + (o - 63 * KB) : wb2 + (o - 126 * KB); };
      sfor<WIN>([&](auto i) { issue(i, win[i]); });
      gemm_steps<4, 32, WIN, false, 0, 32>(acc3, win, issue, addr);
      BARRIER();
      STAMP();
      const f2 ac = ldsr<f2>(L_AC + hw * 256 + t32 * 8);
      const u32 sh = WS_HF + L * 7 * MB + (T13 * 112 + m * 8) * 1024, lc = fresh(L_CS1 + 16 * h);
      constexpr float K1 = -2.0f * 0.7978845608028654f * 1.4426950408889634f, K2 = K1 * 0.044715f;
      sfor<4>([&](auto t) {
        f16v v;
        sfor<4>([&](auto g) {
          const f4 cs = ldsr<f4>(lc + t * 128 + 32 * g), bb = ldsr<f4>(lc + 512 + t * 128 + 32 * g);
          sfor<4>([&](auto i) {
            const float y = __builtin_fmaf(ac.x, acc3[t][4 * g + i], __builtin_fmaf(ac.y, cs[(int)i], bb[(int)i]));
            const float e = __builtin_amdgcn_exp2f(y * __builtin_fmaf(y * y, K2, K1));
            v[4 * g + i] = y * __builtin_amdgcn_rcpf(1.0f + e);
          });
        });
        bst16_sc1<(t & 1) * 2048>(pk8(v, 0), l16, rws, sh + (t >> 1) * 4096); bst16_sc1<(t & 1) * 2048 + 1024>(pk8(v, 8), l16, rws, sh + (t >> 1) * 4096);
      });
    } else { BARRIER(); STAMP(); }
    STAMP();
    WAIT_VM0(); BARRIER();
    STAMP();
    sync(L * 5 + 3);
    BARRIER();
    STAMP();
    {
      constexpr int WIN = 24;
      v4u win[WIN];
      f16v acc2[2] = {};
      const u32 so = WS_HF + L * 7 * MB + (T24 * 112 + kh * 56) * 1024, wb0 = fresh(kh * 2048 + trbase(lane)), wb1 = fresh(wb0 + 60 * KB), wb2 = fresh(wb0 + 120 * KB);
      auto issue = [&](auto i, v4u& d) { bld16<(i & 3) * 1024, true>(d, l16, rws, so + (i >> 2) * 4096); };
      auto addr = [&](auto i) { constexpr int o = ((i / 2 + 32) % 39) * 4096 + (i & 1) * 1024; return o < 60 * KB ? wb0 + o : o < 120 * KB ? wb1 + (o - 60 * KB) : wb2 + (o - 120 * KB); };
      sfor<WIN>([&](auto i) { issue(i, win[i]); });
      gemm_steps<2, 56, WIN, false, 0, 17>(acc2, win, issue, addr);
      BARRIER();
      gemm_steps<2, 56, WIN, false, 17, 39>(acc2, win, issue, addr);
      BARRIER();
      gemm_steps<2, 56, WIN, false, 39, 56>(acc2, win, issue, addr);
      xch_write(acc2, L_X4);
      BARRIER();
      STAMP();
      res_epilogue(acc2, L_X4, true, 1, L == 1);
    }
    if (L == 0) {
      WAIT_VM0(); BARRIER();
      STAMP();
      sync(4);
      BARRIER();
    }
  }
  STAMP();
  if (w == 0 && m == 0 && lane0 == 0) __hip_atomic_store((GAS u64*)(p.ws + WS_EPOCH + grp * 256), tag0, __ATOMIC_RELAXED, __HIP_MEMORY_SCOPE_AGENT);
}

extern "C" void kernel_launch(void* const* d_in, const int* in_sizes, int n_in, void* d_out, int out_size, void* d_ws, size_t ws_size, hipStream_t stream) {
  Params p;
  p.x = d_in[0]; p.wqkv = d_in[1]; p.wo = d_in[2]; p.w1 = d_in[4]; p.b1 = d_in[5]; p.w2 = d_in[6]; p.b2 = d_in[7]; p.out = d_out; p.ws = (unsigned char*)d_ws;
  (void)hipFuncSetAttribute((const void*)fwd, hipFuncAttributeMaxDynamicSharedMemorySize, LDS_BYTES);
  hipLaunchKernelGGL(fwd, dim3(256), dim3(512), LDS_BYTES, stream, p);
}
```

```cpp
#include <hip/hip_runtime.h>
#include <type_traits>
#include <utility>

#define STAMPS 0

#define GAS __attribute__((address_space(1)))
#define LAS __attribute__((address_space(3)))
#define DI __device__ __forceinline__
typedef unsigned u32;
typedef unsigned long long u64;
typedef u32 v4u __attribute__((ext_vector_type(4)));
typedef u32 v2u __attribute__((ext_vector_type(2)));
typedef int v4i __attribute__((ext_vector_type(4)));
typedef short s4 __attribute__((ext_vector_type(4)));
typedef __bf16 bf8 __attribute__((ext_vector_type(8)));
typedef __bf16 bf2 __attribute__((ext_vector_type(2)));
typedef float f2 __attribute__((ext_vector_type(2)));
typedef float f4 __attribute__((ext_vector_type(4)));
typedef float f16v __attribute__((ext_vector_type(16)));
template <int I> using ic = std::integral_constant<int, I>;

template <class F, int... I> DI void sfor_impl(F&& f, std::integer_sequence<int, I...>) { (f(ic<I>{}), ...); }
template <int N, class F> DI void sfor(F&& f) { sfor_impl(f, std::make_integer_sequence<int, N>{}); }

constexpr int KB = 1024, MB = 1024 * 1024;
constexpr int WS_EPOCH = 0;
constexpr int WS_ST0 = 8 * KB;
constexpr int WS_FLAG = 32 * KB;
constexpr int WS_XF = 1 * MB;
constexpr int WS_ST = WS_XF + 6 * MB;
constexpr int WS_KV = WS_ST + 1 * MB;
constexpr int WS_OF = WS_KV + 8 * MB;
constexpr int WS_HF = WS_OF + 4 * MB;
constexpr int WS_STAMP = WS_HF + 14 * MB;
constexpr int WS_END = WS_STAMP + (STAMPS ? MB : 0);
constexpr int L_KV = 0, L_ONES = 48 * KB, L_TA = 54 * KB, L_TB = 58 * KB, L_WQ = 60 * KB, L_WO = 92 * KB, L_W1 = 0, L_XCH = 44 * KB;
constexpr int L_SD = 156 * KB, L_RS = L_SD + 1024, L_CSQ = L_RS + 512, L_CS1 = L_CSQ + 384, L_B1 = L_CS1 + 512, L_ZERO = L_B1 + 512, L_FLG = L_ZERO + 16, LDS_BYTES = 160 * KB;

struct Params { const void *x, *wqkv, *wo, *w1, *b1, *w2, *b2; void* out; unsigned char* ws; };

DI v4i mk_rsrc(const void* p, u32 bytes) { v4i r; r.x = (int)(u32)(u64)p; r.y = (int)((u32)((u64)p >> 32) & 0xffffu); r.z = (int)bytes; r.w = 0x00020000; return r; }
template <int OFF, bool SC1> DI void bld16(v4u& d, u32 voff, v4i rs, u32 soff) {
  if constexpr (SC1) asm volatile("buffer_load_dwordx4 %0, %1, %2, %3 offen offset:%4 sc1" : "=v"(d) : "v"(voff), "s"(rs), "s"(soff), "i"(OFF));
  else asm volatile("buffer_load_dwordx4 %0, %1, %2, %3 offen offset:%4" : "=v"(d) : "v"(voff), "s"(rs), "s"(soff), "i"(OFF));
}
template <int OFF, bool SC1> DI void bld8(v2u& d, u32 voff, v4i rs, u32 soff) {
  if constexpr (SC1) asm volatile("buffer_load_dwordx2 %0, %1, %2, %3 offen offset:%4 sc1" : "=v"(d) : "v"(voff), "s"(rs), "s"(soff), "i"(OFF));
  else asm volatile("buffer_load_dwordx2 %0, %1, %2, %3 offen offset:%4" : "=v"(d) : "v"(voff), "s"(rs), "s"(soff), "i"(OFF));
}
template <int OFF> DI void bst16(v4u d, u32 voff, v4i rs, u32 soff) {
  asm volatile("buffer_store_dwordx4 %0, %1, %2, %3 offen offset:%4 sc1\n\ts_nop 1" ::"v"(d), "v"(voff), "s"(rs), "s"(soff), "i"(OFF));
}
DI void bst8(v2u d, u32 voff, v4i rs, u32 soff) { asm volatile("buffer_store_dwordx2 %0, %1, %2, %3 offen sc1" ::"v"(d), "v"(voff), "s"(rs), "s"(soff)); }
template <bool SC1 = false> DI void dma16(u32 lds, u32 voff, v4i rs, u32 soff) {
  if constexpr (SC1) asm volatile("s_mov_b32 m0, %0\n\ts_nop 0\n\tbuffer_load_dwordx4 %1, %2, %3 offen sc1 lds" ::"s"(lds), "v"(voff), "s"(rs), "s"(soff));
  else asm volatile("s_mov_b32 m0, %0\n\ts_nop 0\n\tbuffer_load_dwordx4 %1, %2, %3 offen lds" ::"s"(lds), "v"(voff), "s"(rs), "s"(soff));
}
template <int N, class T> DI void wait_vm(T& x) { asm volatile("s_waitcnt vmcnt(%1)" : "+v"(x) : "i"(N)); }
template <int N, class T> DI void wait_vm(T& x, T& y) { asm volatile("s_waitcnt vmcnt(%2)" : "+v"(x), "+v"(y) : "i"(N)); }
template <class T> DI void pin(T& x) { asm volatile("" : "+v"(x)); }
#define WAIT_VM(n) asm volatile("s_waitcnt vmcnt(" #n ")" ::: "memory")
#define BARRIER() do { STAMP(); asm volatile("s_waitcnt lgkmcnt(0)\n\ts_barrier" ::: "memory"); } while (0)
#define SBAR() __builtin_amdgcn_sched_barrier(0)
DI f16v mfma(v4u a, v4u b, f16v c) { return __builtin_amdgcn_mfma_f32_32x32x16_bf16(__builtin_bit_cast(bf8, a), __builtin_bit_cast(bf8, b), c, 0, 0, 0); }
DI u32 pk(float lo, float hi) { bf2 r = __builtin_convertvector((f2){lo, hi}, bf2); return __builtin_bit_cast(u32, r); }
DI float blo(u32 v) { return __builtin_bit_cast(float, v << 16); }
DI float bhi(u32 v) { return __builtin_bit_cast(float, v & 0xffff0000u); }
template <class T> DI T ldsr(u32 a) { return *(LAS T*)(size_t)a; }
template <class T> DI void ldsw(u32 a, T v) { *(LAS T*)(size_t)a = v; }
DI v4u trfrag(u32 a) {
  const v2u lo = __builtin_bit_cast(v2u, __builtin_amdgcn_ds_read_tr16_b64_v4i16((LAS s4*)(size_t)a));
  const v2u hi = __builtin_bit_cast(v2u, __builtin_amdgcn_ds_read_tr16_b64_v4i16((LAS s4*)(size_t)(a + 512)));
  return (v4u){lo.x, lo.y, hi.x, hi.y};
}
DI f2 halves(float v) {
  auto r = __builtin_amdgcn_permlane32_swap(__builtin_bit_cast(u32, v), __builtin_bit_cast(u32, v), false, false);
  const u32 a = r[0], b = r[1]; return (f2){__builtin_bit_cast(float, a), __builtin_bit_cast(float, b)};
}
DI float bf(u32 v) { return __builtin_bit_cast(float, v); }
DI u32 fb(float v) { return __builtin_bit_cast(u32, v); }
DI float fadd(float a, float b) { float r; asm("v_add_f32 %0, %1, %2" : "=v"(r) : "v"(a), "v"(b)); return r; }
DI float hsum(float v) { const f2 r = halves(v); return r.x + r.y; }
DI float hmax(float v) { const f2 r = halves(v); return __builtin_fmaxf(r.x, r.y); }
DI float mx3(float a, float b, float c) { return __builtin_fmaxf(__builtin_fmaxf(a, b), c); }
DI float max16(const f16v& s, float m) {
  float a = mx3(m, s[0], s[1]), b = mx3(s[2], s[3], s[4]);
  a = mx3(a, s[5], s[6]); b = mx3(b, s[7], s[8]); a = mx3(a, s[9], s[10]); b = mx3(b, s[11], s[12]); a = mx3(a, s[13], s[14]);
  return mx3(a, b, s[15]);
}
DI v4u pk8(const f16v& v, int o) { return (v4u){pk(v[o], v[o + 1]), pk(v[o + 2], v[o + 3]), pk(v[o + 4], v[o + 5]), pk(v[o + 6], v[o + 7])}; }
#define SPIN_LOG 12
constexpr u32 SPIN_CAP = 1u << SPIN_LOG;
DI u32 fresh(u32 v) { asm volatile("" : "+v"(v)); return v; }
DI u32 sfresh(u32 v) { asm volatile("" : "+s"(v)); return v; }
constexpr u32 EMPTY = 0xffffffffu;
DI u32 umax3(u32 a, u32 b, u32 c) { return max(max(a, b), c); }
DI u32 canon(u32 v) { return min(v, EMPTY - 1); }
DI v4u canon(v4u v) { return (v4u){canon(v.x), canon(v.y), canon(v.z), canon(v.w)}; }
DI bool empty2(const v4u& a, const v4u& b) { return __any(max(umax3(a.x, a.y, a.z), umax3(umax3(a.w, b.x, b.y), b.z, b.w)) == EMPTY); }

#define RETRY_ARG
#define RETRY_PASS
#define RETRY_INC()
template <int P, int N, int NX> DI u32 validate1(v4u (&x)[NX]) {
  v4u &a = x[P % NX], &b = x[(P + 1) % NX];
  wait_vm<N>(a, b);
  return umax3(umax3(a.x, a.y, a.z), a.w, b.x);
}
template <int P, int NX, class FI> DI void validate2(u32 v, v4u (&x)[NX], FI&& issue RETRY_ARG) {
  v4u &a = x[P % NX], &b = x[(P + 1) % NX];
  if (__builtin_expect(__any(max(umax3(v, b.y, b.z), b.w) == EMPTY), 0)) {
#pragma unroll 1
    for (u32 spins = 0; spins < SPIN_CAP; ++spins) {
      __builtin_amdgcn_s_sleep(4); RETRY_INC();
      issue(ic<P>{}, a); issue(ic<P + 1>{}, b);
      wait_vm<0>(a, b);
      if (!empty2(a, b)) break;
    }
  }
}
template <int N> DI void wait_lgkm() { __builtin_amdgcn_s_waitcnt(0xc07f | (N << 8)); }
constexpr int cmin(int a, int b) { return a < b ? a : b; }
constexpr int issued(int s, int KS, int LOOK, int INIT) { return s == 0 ? INIT : cmin(cmin(INIT + 2 * s, ((s - 1) & ~1) + LOOK), KS); }
template <int NT, int KS, int LOOK, int NX, bool SWAPLAST, int S0, int S1, int INIT = LOOK, class FI, class FA>
DI void gemm_steps(f16v* acc, v4u (&x)[NX], FI&& issue, FA&& addr RETRY_ARG) {
  constexpr int DEP = 5, N0 = S0 * NT, N1 = S1 * NT;
  v4u ring[DEP + 1];
  u32 vv;
  sfor<DEP>([&](auto i) { ring[(N0 + i) % (DEP + 1)] = trfrag(addr(ic<N0 + i>{})); });
  SBAR();
  if constexpr (LOOK > 0 && S0 == 0) { vv = validate1<0, INIT - 2>(x); validate2<0>(vv, x, issue RETRY_PASS); }
  sfor<N1 - N0>([&](auto i_) {
    constexpr int i = N0 + i_, s = i / NT, t = i % NT;
    if constexpr ((i_ & 1) == 0 && i + DEP <= N1) { wait_lgkm<2 * DEP - 4>(); SBAR(); }
    if constexpr (SWAPLAST && t == NT - 1) acc[t] = mfma(x[s % NX], ring[i % (DEP + 1)], acc[t]);
    else acc[t] = mfma(ring[i % (DEP + 1)], x[s % NX], acc[t]);
    SBAR();
    if constexpr (i + DEP < N1) ring[(i + DEP) % (DEP + 1)] = trfrag(addr(ic<i + DEP>{}));
    if constexpr (LOOK > 0) {
      constexpr int f0 = issued(s, KS, LOOK, INIT), f1 = issued(s + 1, KS, LOOK, INIT);
      static_assert(NT > 2 || (s & 1) == 0 || f1 == f0, "two-tile loops issue in even steps only");
      if constexpr (t < 2 && f0 + t < f1) { SBAR(); issue(ic<f0 + t>{}, x[(f0 + t) % NX]); }
      if constexpr ((s & 1) == 1 && t == NT - 2 && s + 1 < KS) { SBAR(); vv = validate1<s + 1, f1 - (s + 3)>(x); }
      if constexpr ((s & 1) == 1 && t == NT - 1 && s + 1 < KS) { SBAR(); validate2<s + 1>(vv, x, issue RETRY_PASS); }
    }
    SBAR();
  });
}

#define STAMP() do {} while (0)
#define STAMPCLK(i) do {} while (0)
DI f2 ln_stats(float s1, float s2) {
  const float mean = s1 * (1.0f / 512.0f), var = __builtin_fmaxf(s2 * (1.0f / 512.0f) - mean * mean, 0.0f);
  return (f2){-mean, __builtin_amdgcn_sqrtf(var + 1e-5f)};
}
DI u32 trbase(u32 lane) { return (4 * (lane >> 5) + ((lane & 15) >> 2)) * 64 + ((lane >> 4) & 1) * 32 + (lane & 3) * 8; }
DI float ex2(float x) { return __builtin_amdgcn_exp2f(x); }
DI v4u expk8(const f16v& s, int o) { return (v4u){pk(ex2(s[o]), ex2(s[o + 1])), pk(ex2(s[o + 2]), ex2(s[o + 3])), pk(ex2(s[o + 4]), ex2(s[o + 5])), pk(ex2(s[o + 6]), ex2(s[o + 7]))}; }
DI v4u aux_tok(f2 st, u32 h) {
  const float v = h ? st.y : st.x; const u32 a = pk(v, v) & 0xffffu; const float r = v - blo(a); const u32 b = pk(r, r) & 0xffffu; const float r2 = r - blo(b);
  return (v4u){a | (b << 16), h ? pk(r2, 0.0f) : a, 0u, 0u};
}
DI v4u aux_col(u32 v) { return (v4u){(v & 0xffffu) | (v << 16), v >> 16, 0u, 0u}; }

struct Attn {
  v4u q[2], kf, v0, v1, p0, p1, t0, t1; f16v o[2], nm[2], sc[2]; u32 ka, va, vb, vstr, l16;
  DI void init(u32 lane, int qb) {
    l16 = lane * 16;
    const bool real = (lane & 16) == 0;
    ka = L_KV + qb * 4096 + l16; vstr = real ? 4096u : 512u;
    va = real ? L_KV + qb * 4096 + 2048 + l16 : L_ONES + qb * 512 + (lane & 15) * 16; vb = real ? va + 1024 : va;
  }
  DI f16v addmask() { f16v c; sfor<4>([&](auto g) { const f4 t = ldsr<f4>(L_TA + g * 1024 + l16); sfor<4>([&](auto i) { c[4 * g + i] = t[(int)i]; }); }); return c; }
  template <int D, int MASK, bool QK, bool KN, int PV> DI void stage(u32 a0, u32 a1, u32 kn) {
    const f16v& si = sc[D];
    float e[16];
    sfor<4>([&](auto i) { e[i] = ex2(si[(int)i]); });
    SBAR();
    wait_lgkm<0>();
    if constexpr (QK) sc[1 - D] = mfma(kf, q[1 - D], nm[1 - D]);
    SBAR();
    if constexpr (KN) kf = ldsr<v4u>(kn);
    sfor<4>([&](auto i) { e[4 + i] = ex2(si[4 + i]); });
    SBAR();
    if constexpr (PV == 1) o[1 - D] = mfma(v0, p0, o[1 - D]);
    if constexpr (PV == 2) o[1 - D] = mfma(v0, p0, (f16v){});
    SBAR();
    v0 = ldsr<v4u>(a0);
    sfor<4>([&](auto i) { e[8 + i] = ex2(si[8 + i]); });
    SBAR();
    if constexpr (PV) o[1 - D] = mfma(v1, p1, o[1 - D]);
    SBAR();
    v1 = ldsr<v4u>(a1);
    sfor<4>([&](auto i) { e[12 + i] = ex2(si[12 + i]); });
    SBAR();
    sfor<4>([&](auto i) { p0[(int)i] = pk(e[2 * i], e[2 * i + 1]); });
    sfor<4>([&](auto i) { p1[(int)i] = pk(e[8 + 2 * i], e[9 + 2 * i]); });
    if constexpr (MASK == 1) { pin(t0); pin(t1); p0 &= ~t0; p1 &= ~t1; }
    if constexpr (MASK == 2) { p0 &= t0; p1 &= t1; }
    pin(p0); pin(p1);
    SBAR();
  }
  template <class F> DI void diag(F&& mid) {
    const f16v c = addmask();
    v4u kd[2];
    sfor<2>([&](auto d) { kd[d] = ldsr<v4u>(ka + 8 * 4096 + d * 1024); });
    t0 = ldsr<v4u>(L_TB + l16); t1 = ldsr<v4u>(L_TB + 1024 + l16);
    SBAR();
    sfor<2>([&](auto d) { sc[d] = mfma(kd[d], q[d], c); });
    va = fresh(va + 8 * vstr); vb = fresh(vb + 8 * vstr);
    SBAR();
    sfor<2>([&](auto d) {
      const float mx = hmax(max16(sc[d], sc[d][0]));
      sfor<16>([&](auto r) { nm[d][(int)r] = -mx; });
      pin(nm[d]);
      sc[d] = mfma(kd[d], q[d], nm[d]);
      SBAR();
    });
    stage<0, 1, false, false, 0>(va, vb, 0);
    mid();
    stage<1, 1, false, false, 2>(va + 256, vb + 256, 0);
  }
  template <class F, class G> DI void rest(G&& mid, F&& hint) {
    sc[0] = mfma(ldsr<v4u>(ka + 7 * 4096), q[0], nm[0]); kf = ldsr<v4u>(ka + 7 * 4096 + 1024);
    sfor<8>([&](auto i) {
      constexpr int j = 7 - i;
      va = fresh(va - vstr); vb = fresh(vb - vstr);
      if constexpr (j == 3) mid();
      if constexpr (j == 1) hint();
      stage<0, j == 0 ? 2 : 0, true, (j > 0), j == 7 ? 2 : 1>(va, vb, ka + (j - 1) * 4096);
      stage<1, j == 0 ? 2 : 0, (j > 0), (j > 0), 1>(va + 256, vb + 256, ka + (j - 1) * 4096 + 1024);
    });
    o[1] = mfma(v0, p0, o[1]); o[1] = mfma(v1, p1, o[1]);
  }
  template <int D> DI v4u finish(int jmin) {
    f16v& oo = o[D];
    float big = __builtin_fabsf(oo[0]); sfor<7>([&](auto r) { big += __builtin_fabsf(oo[r + 1]); });
    if (__builtin_expect(!__all(oo[8] <= 0x1p60f && big <= 0x1p126f), 0)) {
      auto scores = [&](int j) {
        f16v c = addmask();
        if (j != 8) sfor<16>([&](auto r) { c[(int)r] = (j == 0 && c[(int)r] == 0.0f) ? -__builtin_inff() : 0.0f; });
        return mfma(ldsr<v4u>(ka + j * 4096 + D * 1024), q[D], c);
      };
      float mx = -__builtin_inff();
#pragma unroll 1
      for (int j = jmin; j <= 8; ++j) mx = max16(scores(j), mx);
      mx = hmax(mx);
      oo = (f16v){};
#pragma unroll 1
      for (int j = jmin; j <= 8; ++j) {
        const f16v s = scores(j) - mx;
        oo = mfma(ldsr<v4u>(va + j * vstr + D * 256), expk8(s, 0), oo); oo = mfma(ldsr<v4u>(vb + j * vstr + D * 256), expk8(s, 8), oo);
      }
    }
    const float rl = __builtin_amdgcn_rcpf(oo[8]);
    sfor<8>([&](auto r) { oo[(int)r] *= rl; });
    return canon(pk8(oo, 0));
  }
};

constexpr int w2_kstep(int i) { return ((i % 14) / 2) * 8 + 2 * (i / 14) + (i & 1); }
constexpr int w2r_slot(int i) { return i < 6 ? 6 + 2 * i : 38 + 2 * (i - 6); }
constexpr int w2_pos(int i) { return i < 38 ? (i + 32) % 38 : (i - 6) % 39; }

__global__ void __launch_bounds__(512, 2) fwd(Params p) {
  extern __shared__ __attribute__((aligned(16))) unsigned char smem[];
  const int w0 = __builtin_amdgcn_readfirstlane(threadIdx.x >> 6);
#define IDS() const int w = sfresh(w0), bid = sfresh(blockIdx.x), grp = (bid & 7) * 2 + (bid >> 7), m = (bid >> 3) & 15, cu = grp * 16 + m, hw = w & 3, cb = m & 7, th = m >> 3, \
              rt = w & 1, kh = (w >> 1) & 1, T13 = 4 * grp + hw, T24 = 4 * grp + 2 * th + rt, rot = 4 * (m & 1) + 2 * (grp & 1) + ((m >> 1) & 1), rot2 = (th + 2 * grp + cb) & 3, rot4 = (cb + 2 * th + 4 * (grp & 1)) % 7; (void)rot; (void)rot2; (void)rot4; (void)cu; (void)cb; (void)kh; (void)T13; (void)T24
  const v4i rws = mk_rsrc(p.ws, WS_END);
  const u32 lane0 = threadIdx.x & 63;
  STAMP(); STAMPCLK(0);
  v2u tagv;
  auto load_tags = [&]() { IDS(); const u32 g = grp + (lane0 < 3 ? lane0 : 0); bld8<0, true>(tagv, (g < 16 ? g : grp) * 256, rws, WS_EPOCH); };
  auto fix_tags = [&]() { pin(tagv); tagv = __builtin_bit_cast(v2u, __builtin_bit_cast(u64, tagv) + 1); };
  auto tag0 = [&]() { return (v2u){(u32)__builtin_amdgcn_readfirstlane(tagv.x), (u32)__builtin_amdgcn_readfirstlane(tagv.y)}; };
  auto publish = [&](int sid) { IDS(); const u32 lane = fresh(lane0); if (lane < 16) bst8(tag0(), lane * 128 + m * 8, rws, WS_FLAG + (sid * 256 + grp * 16) * 128); };
  auto await = [&](int sid, u32 first, u32 mask) {
    IDS(); const u64 t0 = __builtin_bit_cast(u64, tag0()); const u32 vo = (fresh(lane0) & mask) * 8;
#pragma unroll 1
    for (u32 spins = 0; spins < SPIN_CAP; ++spins) {
      v2u f; bld8<0, true>(f, vo, rws, WS_FLAG + (sid * 256 + cu) * 128 + first * 8); wait_vm<0>(f);
      if (__all(__builtin_bit_cast(u64, f) == t0)) break;
      __builtin_amdgcn_s_sleep(2);
    }
  };
  const u32 wrow = lane0 >> 2, wch = (lane0 & 3) * 16;
  if (w0 >= 4) {
    const v4i ro = mk_rsrc(p.wo, 512 * KB), r1 = mk_rsrc(p.w1, 1792 * KB), r2 = mk_rsrc(p.w2, 1792 * KB);
    u32 vo24, vo1;
    { IDS(); vo24 = wrow * 1024 + wch + cb * 128; vo1 = wrow * 3584 + wch + m * 256; }
    auto stage_w1 = [&](int s0, int s1) {
      IDS();
      if (m >= 14) return;
#pragma unroll 1
      for (int s = s0 + hw; s < s1; s += 4) {
        const u32 so = ((s + 17 + 4 * rot) & 31) * 16 * 3584;
        dma16(L_W1 + s * 4096, vo1, r1, so); dma16(L_W1 + s * 4096 + 1024, vo1, r1, so + 64); dma16(L_W1 + s * 4096 + 2048, vo1, r1, so + 128); dma16(L_W1 + s * 4096 + 3072, vo1, r1, so + 192);
      }
    };
    auto w2_row = [&](int i, int rot4) { const int t = (i * 37) >> 9, r = i - 14 * t, j = (r >> 1) + rot4; return (u32)(((j >= 7 ? j - 7 : j) * 8 + 2 * t + (r & 1)) * 16384); };
    auto stage_w2 = [&](int i0, int i1, int p0) {
      IDS();
#pragma unroll 1
      for (int i = i0 + hw; i < i1; i += 4) {
        const u32 la = (i + p0) * 4096, so = w2_row(i, rot4);
        dma16(la, vo24, r2, so); dma16(la + 1024, vo24, r2, so + 64); dma16(la + 2048, vo24, r2, so + 56 * 16384); dma16(la + 3072, vo24, r2, so + 56 * 16384 + 64);
      }
    };
    const v4u ones = {0x3f803f80u, 0x3f803f80u, 0x3f803f80u, 0x3f803f80u}, empt = {EMPTY, EMPTY, EMPTY, EMPTY};
    auto tables = [&]() {
      IDS(); const u32 lane = fresh(lane0), l16 = lane * 16; const int dq = (int)(lane & 31) - 4 * (int)(lane >> 5);
      sfor<4>([&](auto g) { f4 t; sfor<4>([&](auto i) { t[(int)i] = 8 * g + i <= dq ? 0.0f : -__builtin_inff(); }); ldsw<f4>(L_TA + g * 1024 + l16, t); });
      sfor<2>([&](auto g) { v4u b; sfor<4>([&](auto d) { const int c = 16 * g + 8 * (d >> 1) + 2 * (d & 1); b[(int)d] = (c > dq ? 0xffffu : 0u) | (c + 1 > dq ? 0xffff0000u : 0u); });
                            ldsw<v4u>(L_TB + g * 1024 + l16, b); });
      sfor<6>([&](auto g) { ldsw<v4u>(L_ONES + g * 1024 + l16, grp + g / 2 >= 2 ? ones : (v4u){}); });
    };
    auto cs_write = [&](const f16v& a, u32 dst) {
      const u32 lane = fresh(lane0);
      if ((lane & 31) == 0) sfor<4>([&](auto g) { v4u q; sfor<4>([&](auto i) { const float v = a[4 * g + i]; const u32 hi = pk(v, v) & 0xffffu; q[(int)i] = hi | (pk(0.0f, v - blo(hi)) & 0xffff0000u); });
                                                  ldsw<v4u>(dst + (8 * g + 4 * (lane >> 5)) * 4, q); });
    };
    auto put_stats = [&](f2 a, f2 b, u32 tok2) { ldsw<f4>(L_SD + tok2 * 16, (f4){a.x, a.y, b.x, b.y}); ldsw<f2>(L_RS + tok2 * 8, (f2){__builtin_amdgcn_rcpf(a.y), __builtin_amdgcn_rcpf(b.y)}); };
    auto fill_h = [&](int L) {
      IDS();
      if (m < 14) { const u32 so = WS_HF + L * 7 * MB + (T13 * 112 + m * 8) * 1024, l16 = fresh(lane0) * 16;
#pragma unroll 1
                    for (int i = 0; i < 8; ++i) { bst16<0>(empt, l16, rws, so + i * 1024); WAIT_VM(3); } }
    };
    v4u keep[24], w2r[30];
    {
      IDS();
      const u32 l16 = lane0 * 16;
      if (hw < 3) {
        const v4i rq = mk_rsrc(p.wqkv, 1536 * KB);
        const u32 vo = (8 * ((wrow >> 2) & 1) + 4 * (wrow >> 3) + (wrow & 3)) * 3072 + wch + m * 64 + hw * 1024;
        auto chunk = [&](int c) { const u32 so = 64 * ((c + rot) & 7) * 3072, la = L_WQ + c * 12288 + hw * 1024; sfor<4>([&](auto i) { dma16(la + i * 3072, vo, rq, so + i * 16 * 3072); }); };
        chunk(0); chunk(1);
        BARRIER();
        f16v a = {}; const u32 b0 = L_WQ + hw * 1024 + trbase(lane0);
        auto landed = [&](int c) {
          ldsw<u32>(L_FLG + hw * 4, c + 1);
          v4u q[4]; sfor<4>([&](auto i) { q[i] = trfrag(b0 + (4 * c + i) * 3072); }); SBAR();
          sfor<4>([&](auto i) { a = mfma(q[i], ones, a); });
        };
        WAIT_VM(4); ldsw<u32>(L_FLG + hw * 4, 1);
        chunk(2);
        landed(0);
#pragma unroll 1
        for (int c = 1; c < 6; ++c) { chunk(c + 2); WAIT_VM(8); landed(c); }
        WAIT_VM(4); landed(6);
        load_tags();
        WAIT_VM(1); landed(7);
        cs_write(a, L_CSQ + hw * 128);
        WAIT_VM(0); fix_tags();
      } else {
        v4u xr[8];
        const v4i rx = mk_rsrc(p.x, 2 * MB); const u32 vx = (lane0 >> 3) * 1024 + (lane0 & 7) * 16, so = (grp * 128 + m * 8) * 1024;
        sfor<8>([&](auto i) { bld16<i * 128, false>(xr[i], vx, rx, so); });
        if (lane0 < 4) ldsw<u32>(L_FLG + lane0 * 4, lane0 == 3 ? 255u : 0u);
        BARRIER();
        load_tags();
        tables();
        WAIT_VM(0); fix_tags();
        sfor<8>([&](auto i) { pin(xr[i]); });
        float s1 = 0.f, s2 = 0.f;
        sfor<32>([&](auto i) { const u32 eu = xr[i / 4][i % 4]; const bf2 e = __builtin_bit_cast(bf2, eu);
                               s1 = __builtin_amdgcn_fdot2_f32_bf16(e, __builtin_bit_cast(bf2, 0x3f803f80u), s1, false); s2 = __builtin_amdgcn_fdot2_f32_bf16(e, e, s2, false); });
        sfor<3>([&](auto i) { s1 += __shfl_xor(s1, 1 << i); s2 += __shfl_xor(s2, 1 << i); });
        if ((lane0 & 7) == 0) bst8(__builtin_bit_cast(v2u, ln_stats(s1, s2)), lane0, rws, WS_ST0 + grp * 1024 + m * 64);
        sfor<4>([&](auto i) { const u32 sf = WS_OF + ((4 * grp + i) * 32 + 2 * m) * 1024; bst16<0>(empt, l16, rws, sf); bst16<1024>(empt, l16, rws, sf); });
        WAIT_VM(0);
        publish(2);
        __builtin_amdgcn_s_sleep(32);
        await(2, 0, 15);
        v4u q; bld16<0, true>(q, l16, rws, WS_ST0 + grp * 1024); wait_vm<0>(q);
        const f4 s = __builtin_bit_cast(f4, q); put_stats((f2){s[0], s[1]}, (f2){s[2], s[3]}, lane0);
      }
    }
    sfor<2>([&](auto L_) {
      constexpr int L = L_;
      IDS();
      const u32 lane = fresh(lane0), l16 = lane * 16;
      auto gather_stats = [&](int buf) {
        __builtin_amdgcn_s_setprio(3);
        v4u q[4]; const u32 so = WS_ST + buf * 256 * KB + T13 * 4096;
#pragma unroll 1
        for (u32 spins = 0; spins < SPIN_CAP; ++spins) {
          bld16<0, true>(q[0], l16, rws, so); bld16<1024, true>(q[1], l16, rws, so); bld16<2048, true>(q[2], l16, rws, so); bld16<3072, true>(q[3], l16, rws, so);
          wait_vm<0>(q[0], q[1]); wait_vm<0>(q[2], q[3]);
          if (!empty2(q[0], q[1]) && !empty2(q[2], q[3])) break;
          __builtin_amdgcn_s_sleep(4);
        }
        float s[4];
        sfor<4>([&](auto i) { const u32 a = q[0][(int)i], b = q[1][(int)i], c = q[2][(int)i], d = q[3][(int)i];
                              float v = fadd(fadd(bf(a), bf(b)), fadd(bf(c), bf(d)));
                              auto r = __builtin_amdgcn_permlane16_swap(fb(v), fb(v), false, false); const u32 r0 = r[0], r1 = r[1];
                              const f2 hh = halves(fadd(bf(r0), bf(r1))); s[i] = fadd(hh.x, hh.y); });
        if (lane < 16) put_stats(ln_stats(s[0], s[1]), ln_stats(s[2], s[3]), hw * 16 + lane);
        __builtin_amdgcn_s_setprio(0);
      };
      auto fill_x = [&](int buf) { const u32 so = WS_XF + buf * 2 * MB + (T24 * 32 + cb * 4 + kh * 2) * 1024; bst16<0>(empt, l16, rws, so); bst16<1024>(empt, l16, rws, so);
                                   if (lane < 16) bst16<0>(empt, l16, rws, WS_ST + buf * 256 * KB + (T24 * 16 + cb * 2 + kh) * 256); };
      auto stage_wo = [&](int s0, int s1) {
#pragma unroll 1
        for (int s = s0 + hw; s < s1; s += 4) { const u32 so = ((s & 16) + ((s + 4 * rot2) & 15)) * 16384; dma16(L_WO + s * 2048, vo24, ro, so); dma16(L_WO + s * 2048 + 1024, vo24, ro, so + 64); } };
      auto w2_get = [&](v4u* r, int s0) { const u32 so = w2_row(s0, rot4) + (hw & 1) * 16384 + (hw >> 1) * 56 * 16384; bld16<0, false>(r[0], vo24, r2, so); bld16<64, false>(r[1], vo24, r2, so); };
      auto w2_set = [&](v4u* r, int s0, u32 ta) { pin(r[0]); pin(r[1]); const u32 o = ta + ((hw & 1) ? w2_pos(s0 + 1) : w2_pos(s0)) * 4096; ldsw<v4u>(o, r[0]); ldsw<v4u>(o + 1024, r[1]); };
      auto w2_regs = [&](auto i0, auto n) { sfor<n>([&](auto j) { constexpr int i = i0 + j; w2_get(&w2r[2 * i], w2r_slot(i)); }); };
      auto w2_put = [&](auto i0, auto n) { const u32 ta = fresh(l16 + (hw >> 1) * 2048); sfor<n>([&](auto j) { constexpr int i = i0 + j; w2_set(&w2r[2 * i], w2r_slot(i), ta); }); };
      v4u st1[24];
      auto w1_pos = [&](int i) { const int j = hw + 4 * i; return j < 11 ? j : j == 11 ? 23 : j < 16 ? j - 1 : j + 8; };
      auto w1_get = [&](auto i0, auto n) { if (m < 14) sfor<n>([&](auto j) { constexpr int i = i0 + j; const u32 so = ((w1_pos(i) + 17 + 4 * rot) & 31) * 16 * 3584;
                                                                             sfor<4>([&](auto t) { bld16<t * 64, false>(st1[4 * i + t], vo1, r1, so); }); }); };
      auto w1_put = [&](auto i0, auto n) { if (m < 14) sfor<n>([&](auto j) { constexpr int i = i0 + j; const u32 o = fresh(l16) + L_W1 + w1_pos(i) * 4096;
                                                                             sfor<4>([&](auto t) { pin(st1[4 * i + t]); ldsw<v4u>(o + t * 1024, st1[4 * i + t]); }); }); };
      f16v cs = {};
      auto w1_colsum = [&](int p0, int p1) {
        const u32 b0 = L_W1 + hw * 1024 + trbase(lane);
#pragma unroll 1
        for (int z = p0; z < p1; z += 4) { v4u q[4]; sfor<4>([&](auto i) { q[i] = trfrag(b0 + (z + i < p1 ? z + i : p0) * 4096); }); SBAR();
                                           sfor<4>([&](auto i) { cs = mfma(q[i], z + i < p1 ? ones : (v4u){}, cs); }); }
      };
      auto halo = [&]() {
        __builtin_amdgcn_s_setprio(3);
        if (4 * grp + 2 * hw >= 8) {
          await(L, 2 * hw, 1);
          sfor<8>([&](auto i) { dma16<true>(L_KV + hw * 8192 + i * 1024, l16, rws, WS_KV + L * 4 * MB + ((4 * grp - 8 + 2 * hw + (i >> 2)) * 16 + m) * 4096 + (i & 3) * 1024); });
        } else sfor<8>([&](auto i) { ldsw<v4u>(L_KV + hw * 8192 + i * 1024 + l16, (v4u){}); });
        __builtin_amdgcn_s_setprio(0);
      };
      BARRIER();
      if (L == 0) sfor<24>([&](auto i) { keep[i] = ldsr<v4u>(L_WQ + hw * 24 * KB + i * 1024 + l16); });
      else if (hw == 3) tables();
      BARRIER();
      if (L == 0) w1_get(ic<3>{}, ic<2>{});
      halo();
      fill_x(2 * L);
      WAIT_VM(3); BARRIER();
      stage_wo(0, 32);
      WAIT_VM(16);
      if (L == 1) w1_get(ic<3>{}, ic<2>{});
      BARRIER();
      stage_w1(15, 23);
      w1_get(ic<0>{}, ic<3>{});
      WAIT_VM(0);
      BARRIER();
      w1_get(ic<5>{}, ic<1>{});
      w1_put(ic<0>{}, ic<2>{});
      if (hw < 3) w1_put(ic<2>{}, ic<1>{});
      BARRIER();
      if (L == 0) {
        if (m < 14) {
          w1_colsum(0, 11); w1_colsum(15, 23);
          if (hw == 0) { const u32 v = ((const GAS u32*)p.b1)[m * 64 + lane]; ldsw<v2u>(L_B1 + lane * 8, (v2u){(v & 0xffffu) | (v << 16), (v >> 16) | (v & 0xffff0000u)}); }
        }
      }
      WAIT_VM(0);
      BARRIER();
      w1_put(ic<4>{}, ic<2>{});
      if (hw == 3) w1_put(ic<2>{}, ic<1>{});
      BARRIER();
      gather_stats(2 * L);
      w1_put(ic<3>{}, ic<1>{});
      if (L == 0 && m < 14) w1_colsum(23, 32);
      BARRIER();
      if (L == 0 && m < 14) { w1_colsum(11, 15); cs_write(cs, L_CS1 + hw * 128); }
      stage_w2(0, 6, 32);
      v4u st2[20];
      if (L == 0) w2_regs(ic<0>{}, ic<6>{}); else sfor<6>([&](auto i) { w2_get(&st2[2 * i], 18 + 2 * i); });
      BARRIER();
      if (L == 0) w2_regs(ic<6>{}, ic<4>{}); else sfor<4>([&](auto i) { w2_get(&st2[12 + 2 * i], 30 + 2 * i); });
      WAIT_VM(8);
      BARRIER();
      w2_put(ic<0>{}, ic<6>{});
      if constexpr (L == 0) {
        stage_w2(18, 26, -6);
        fill_x(1); const u32 so = WS_OF + 2 * MB + (T13 * 32 + 2 * m) * 1024; bst16<0>(empt, l16, rws, so); bst16<1024>(empt, l16, rws, so);
        WAIT_VM(0);
        BARRIER();
        stage_w2(26, 38, -6);
        w2_regs(ic<10>{}, ic<5>{});
        BARRIER();
        WAIT_VM(0);
      } else {
        WAIT_VM(0);
        const u32 ta = fresh(l16 + (hw >> 1) * 2048);
        sfor<10>([&](auto i) { w2_set(&st2[2 * i], 18 + 2 * i, ta); });
        BARRIER();
      }
      BARRIER();
      w2_put(ic<6>{}, ic<9>{});
      BARRIER();
      const u32 ka = fresh(L_WQ + hw * 24 * KB + (8 * ((lane >> 4) & 1) + 4 * (lane >> 5) + ((lane >> 2) & 3)) * 64 + (lane & 3) * 16);
      if (L == 0) { if (hw < 2) sfor<24>([&](auto i) { ldsw<v4u>(ka + i * 1024, keep[i]); }); else fill_h(1); }
      BARRIER();
      if constexpr (L == 0) {
        if (hw >= 2) sfor<24>([&](auto i) { ldsw<v4u>(ka + i * 1024, keep[i]); });
        BARRIER();
        gather_stats(1);
        if (hw < 2) fill_h(1);
      }
    });
    STAMP();
    return;
  }
  v4u res[2], b2p[2];
  v4u qf[2];
  struct Aux { v4u tf; u32 c[4]; float rs, sd; f4 rv[4]; };
  auto aux_read = [&](Aux& x, bool ffn) {
    IDS();
    const u32 lane = fresh(lane0), h = lane >> 5, t32 = lane & 31;
    const f2 st = ldsr<f2>(L_SD + hw * 256 + t32 * 8);
    x.rs = ldsr<float>(L_RS + hw * 128 + t32 * 4);
    if (ffn) { const u32 lc = fresh(L_CS1 + h * 512 + t32 * 4); sfor<4>([&](auto t) { x.c[t] = ldsr<u32>(lc + t * 128); }); }
    else { const u32 lc = fresh(L_CSQ + t32 * 4), lr = fresh(L_RS + hw * 128 + 16 * h);
           sfor<3>([&](auto t) { const u32 v = ldsr<u32>(lc + t * 128); x.c[t] = h ? 0u : v; });
           sfor<4>([&](auto g) { x.rv[g] = ldsr<f4>(lr + 32 * g); }); }
    x.tf = aux_tok(st, h); x.sd = st.y;
  };
  auto kv_epilogue = [&](int L, const Aux& x, f16v& ak, f16v& av) {
    IDS();
    const u32 l16 = fresh(lane0) * 16;
    ak = mfma(aux_col(x.c[1]), x.tf, ak); av = mfma(x.tf, aux_col(x.c[2]), av);
    const u32 so = WS_KV + L * 4 * MB + (T13 * 16 + m) * 4096, lk = fresh(L_KV + (8 + hw) * 4096 + l16);
    ak *= x.rs;
    const v4u k0 = pk8(ak, 0), k1 = pk8(ak, 8); bst16<0>(k0, l16, rws, so); bst16<1024>(k1, l16, rws, so);
    sfor<16>([&](auto r) { av[(int)r] *= x.rv[r / 4][r % 4]; });
    const v4u v0 = pk8(av, 0), v1 = pk8(av, 8); bst16<2048>(v0, l16, rws, so); bst16<3072>(v1, l16, rws, so);
    ldsw<v4u>(lk, k0); ldsw<v4u>(lk + 1024, k1); ldsw<v4u>(lk + 2048, v0); ldsw<v4u>(lk + 3072, v1);
  };
  auto q_epilogue = [&](const Aux& x, f16v aq) { aq *= x.rs * (0.25f * 1.4426950408889634f); qf[0] = pk8(aq, 0); qf[1] = pk8(aq, 8); };
  auto kv_flag = [&](int L) {
    IDS(); const u32 lane = fresh(lane0);
    WAIT_VM(0);
    if ((lane == 1 || lane == 2) && grp + lane < 16) bst8(tagv, lane * (16 * 128 - 32) + 64 + hw * 8, rws, WS_FLAG + (L * 256 + cu) * 128);
  };
  {
    IDS();
    f16v acc1[3] = {};
    const u32 lane = lane0, h = lane >> 5, t32 = lane & 31;
    const v4i rx = mk_rsrc(p.x, 2 * MB);
    const u32 vo = wrow * 1024 + (((lane & 3) ^ ((lane >> 4) & 3)) * 16), so = T13 * 32 * 1024, xs = hw * 12 * KB;
    auto issue = [&](int u) { const u32 o = u < 16 ? so + ((u + 2 * rot) & 15) * 64 : T24 * 32 * 1024 + cb * 128 + kh * 64;
                              dma16(xs + (u % 6) * 2048, vo, rx, o); dma16(xs + (u % 6) * 2048 + 1024, vo, rx, o + 16384); };
    issue(0); issue(1);
    BARRIER();
    load_tags();
    v2u bb[4];
    { const v4i rb = mk_rsrc(p.b2, 1024); const u32 bo = (cb * 64 + kh * 32 + 4 * h) * 2; sfor<4>([&](auto i) { bld8<i * 16, false>(bb[i], bo, rb, 0); }); }
    issue(2); issue(3);
    const u32 xa0 = xs + t32 * 64 + ((h ^ ((t32 >> 2) & 3)) * 16), xa1 = xa0 ^ 32;
    const u32 wb0 = fresh(L_WQ + trbase(lane)), wb1 = fresh(wb0 + 48 * KB);
    auto addr = [&](auto i) { constexpr int o = i * 1024; return o < 48 * KB ? wb0 + o : wb1 + (o - 48 * KB); };
    auto chunk_wait = [&](u32 c) {
#pragma unroll 1
      for (u32 spins = 0; spins < SPIN_CAP; ++spins) { const v4u f = *(volatile LAS v4u*)(size_t)L_FLG; if ((u32)__builtin_amdgcn_readfirstlane(min(min(f.x, f.y), min(f.z, f.w))) > c) break; }
      asm volatile("" ::: "memory");
    };
    v4u xf[4], ring[5];
    WAIT_VM(11);
    xf[0] = ldsr<v4u>(xa0); xf[1] = ldsr<v4u>(xa1);
    chunk_wait(0);
    sfor<4>([&](auto j) { ring[j] = trfrag(addr(j)); });
    SBAR();
    sfor<96>([&](auto i_) {
      constexpr int i = i_, s = i / 3, t = i % 3, u = s / 2;
      if constexpr (t == 2) acc1[t] = mfma(xf[s % 4], ring[i % 5], acc1[t]); else acc1[t] = mfma(ring[i % 5], xf[s % 4], acc1[t]);
      SBAR();
      if constexpr (i + 4 < 96) { if constexpr ((i + 4) % 12 == 0) chunk_wait((i + 4) / 12); ring[(i + 4) % 5] = trfrag(addr(ic<i + 4>{})); }
      if constexpr (i % 6 == 0 && u + 4 < 17) { SBAR(); issue(u + 4); }
      if constexpr (i % 6 == 1 && u + 1 < 16) {
        asm volatile("s_waitcnt vmcnt(%0)" ::"i"(2 * ((u + 4 < 16 ? u + 4 : 16) - (u + 1))) : "memory");
        xf[(s + 2) % 4] = ldsr<v4u>(xa0 + ((u + 1) % 6) * 2048); xf[(s + 3) % 4] = ldsr<v4u>(xa1 + ((u + 1) % 6) * 2048);
      }
      SBAR();
    });
    WAIT_VM(0);
    fix_tags();
    sfor<4>([&](auto i) { pin(bb[i]); });
    sfor<2>([&](auto f) { const u32 ra = xs + (16 % 6) * 2048 + t32 * 64 + 8 * h, z = ((t32 >> 2) & 3) * 16;
                          const v2u a = ldsr<v2u>(ra + ((2 * f * 16) ^ z)), b = ldsr<v2u>(ra + (((2 * f + 1) * 16) ^ z));
                          res[f] = (v4u){a.x, a.y, b.x, b.y}; b2p[f] = (v4u){bb[2 * f].x, bb[2 * f].y, bb[2 * f + 1].x, bb[2 * f + 1].y}; });
    BARRIER();
    Aux ax; aux_read(ax, false);
    SBAR();
    acc1[0] = mfma(aux_col(ax.c[0]), ax.tf, acc1[0]);
    kv_epilogue(0, ax, acc1[1], acc1[2]);
    q_epilogue(ax, acc1[0]);
  }
  for (int L = 0;; ++L) {
    IDS();
    const u32 lane = fresh(lane0), l16 = lane * 16, h = lane >> 5, t32 = lane & 31;
    auto res_init = [&](f16v (&acc)[2], bool bias) {
      sfor<8>([&](auto i) { const u32 r = res[i / 4][i % 4], b = bias ? fresh(b2p[i / 4][i % 4]) : 0u;
                            acc[0][2 * i] = bias ? blo(r) + blo(b) : blo(r); acc[0][2 * i + 1] = bias ? bhi(r) + bhi(b) : bhi(r); });
      acc[1] = (f16v){};
      pin(acc[0]);
    };
    auto res_epilogue = [&](f16v (&acc)[2], int buf, int sid, bool last) {
      const u32 xa = fresh(L_XCH + w * 4096 + l16), xb = fresh(L_XCH + (w ^ 2) * 4096 + l16);
      const u32 so = sfresh(WS_XF + buf * 2 * MB + (T24 * 32 + cb * 4 + kh * 2) * 1024), ss = sfresh(WS_ST + buf * 256 * KB + (T24 * 16 + cb * 2 + kh) * 256);
      sfor<4>([&](auto g) { ldsw<f4>(xa + g * 1024, (f4){acc[1][4 * g], acc[1][4 * g + 1], acc[1][4 * g + 2], acc[1][4 * g + 3]}); });
      BARRIER();
      f4 o[4]; sfor<4>([&](auto g) { o[g] = ldsr<f4>(xb + g * 1024); });
      f16v v; sfor<16>([&](auto r) { v[(int)r] = acc[0][(int)r] + o[r / 4][r % 4]; });
      res[0] = canon(pk8(v, 0)); res[1] = canon(pk8(v, 8));
      if (!last) {
        bst16<0>(res[0], l16, rws, so); bst16<1024>(res[1], l16, rws, so);
        SBAR();
        float s1 = 0.f, s2 = 0.f;
        sfor<8>([&](auto i) { const u32 eu = res[i / 4][i % 4]; const bf2 e = __builtin_bit_cast(bf2, eu);
                              s1 = __builtin_amdgcn_fdot2_f32_bf16(e, __builtin_bit_cast(bf2, 0x3f803f80u), s1, false); s2 = __builtin_amdgcn_fdot2_f32_bf16(e, e, s2, false); });
        s1 = hsum(s1); s2 = hsum(s2);
        bst8((v2u){canon(fb(s1)), canon(fb(s2))}, t32 * 8, rws, ss);
        if (w == 0) await(sid, 0, 15); else WAIT_VM(0);
        BARRIER();
      } else {
        GAS unsigned char* op = (GAS unsigned char*)p.out + (T24 * 32 + t32) * 1024 + (cb * 64 + kh * 32 + 8 * h) * 2;
        sfor<2>([&](auto f) {
          auto a = __builtin_amdgcn_permlane32_swap(res[f].x, res[f].z, false, false), b = __builtin_amdgcn_permlane32_swap(res[f].y, res[f].w, false, false);
          *(GAS v4u*)(op + f * 32) = (v4u){a[0], b[0], a[1], b[1]}; });
      }
    };
    BARRIER();
    {
      Attn at; at.init(lane, hw); at.q[0] = qf[0]; at.q[1] = qf[1];
      at.diag([&]() { if (L == 0) kv_flag(0); });
      BARRIER();
      at.rest([&]() { BARRIER();   }, [&]() { if (w == 1) publish(3 + L); });
      const u32 so = WS_OF + L * 2 * MB + (T13 * 32 + 2 * m) * 1024; const int jmin = max(8 - T13, 0);
      bst16<0>(at.finish<0>(jmin), l16, rws, so); bst16<1024>(at.finish<1>(jmin), l16, rws, so);
    }
    BARRIER();
    if (w == 0) await(3 + L, 0, 15); else WAIT_VM(0);
    BARRIER();
    {
      constexpr int LOOK = 10;
      v4u win[LOOK];
      f16v acc2[2];
      const u32 so = WS_OF + L * 2 * MB + (T24 * 32 + kh * 16) * 1024, wb = fresh(L_WO + kh * 33 * KB + trbase(lane)), wc = fresh(wb ^ 1024);
      auto issue = [&](auto i, v4u& d) { bld16<(i & 3) * 1024, true>(d, l16, rws, so + (((i >> 2) + rot2) & 3) * 4096); };
      auto addr = [&](auto i) { return ((i & 1) ? wc : wb) + (i / 2) * 2048; };
      sfor<LOOK>([&](auto i) { issue(i, win[i]); });
      res_init(acc2, false);
      gemm_steps<2, 16, LOOK, LOOK, false, 0, 12>(acc2, win, issue, addr RETRY_PASS);
      if (w == 1) publish(5 + L);
      gemm_steps<2, 16, LOOK, LOOK, false, 12, 16>(acc2, win, issue, addr RETRY_PASS);
      res_epilogue(acc2, 2 * L, 5 + L, false);
    }
    const u32 soh = WS_HF + L * 7 * MB + (T24 * 112 + kh * 56) * 1024;
    if (m < 14) {
      constexpr int LOOK = 20;
      v4u win[LOOK];
      f16v acc3[4] = {};
      const u32 so = WS_XF + 2 * L * 2 * MB + T13 * 32 * 1024, wb0 = fresh(L_W1 + trbase(lane)), wb1 = fresh(wb0 + 63 * KB), wb2 = fresh(wb0 + 126 * KB);
      auto issue = [&](auto i, v4u& d) { bld16<(i & 3) * 1024, true>(d, l16, rws, so + (((i >> 2) + rot) & 7) * 4096); };
      auto addr = [&](auto i) { constexpr int o = ((i + 60) & 127) * 1024; return o < 63 * KB ? wb0 + o : o < 126 * KB ? wb1 + (o - 63 * KB) : wb2 + (o - 126 * KB); };
      sfor<8>([&](auto i) { issue(i, win[i]); });
      gemm_steps<4, 32, LOOK, LOOK, false, 0, 14, 8>(acc3, win, issue, addr RETRY_PASS);
      BARRIER();
      gemm_steps<4, 32, LOOK, LOOK, false, 14, 24, 8>(acc3, win, issue, addr RETRY_PASS);
      BARRIER();
      Aux ax; aux_read(ax, true);
      gemm_steps<4, 32, LOOK, LOOK, false, 24, 32, 8>(acc3, win, issue, addr RETRY_PASS);
      sfor<4>([&](auto t) { acc3[t] = mfma(aux_col(ax.c[t]), ax.tf, acc3[t]); });
      BARRIER();
      const float rs = ax.rs, sd = ax.sd;
      const u32 sh = WS_HF + L * 7 * MB + (T13 * 112 + m * 8) * 1024;
      constexpr float K1 = -2.0f * 0.7978845608028654f * 1.4426950408889634f, K2 = K1 * 0.044715f;
      const float c1 = K1 * rs, c2 = K2 * rs * rs * rs;
      sfor<8>([&](auto f) {
        v4u q;
        sfor<4>([&](auto d) {
          const f2 u = {acc3[f / 2][(f & 1) * 8 + 2 * d], acc3[f / 2][(f & 1) * 8 + 2 * d + 1]};
          const f2 z = u * __builtin_elementwise_fma(u * u, (f2){c2, c2}, (f2){c1, c1});
          const f2 dn = __builtin_elementwise_fma((f2){ex2(z.x), ex2(z.y)}, (f2){sd, sd}, (f2){sd, sd});
          const f2 g = u * (f2){__builtin_amdgcn_rcpf(dn.x), __builtin_amdgcn_rcpf(dn.y)};
          q[(int)d] = canon(pk(g.x, g.y));
        });
        bst16<(f & 3) * 1024>(q, l16, rws, sh + (f >> 2) * 4096);
      });
    } else {
      BARRIER(); BARRIER(); BARRIER();
      v4u a, b;
#pragma unroll 1
      for (u32 spins = 0; spins < SPIN_CAP && L == 1; ++spins) {
        __builtin_amdgcn_s_sleep(8);
        bld16<2048, true>(a, l16, rws, soh); bld16<3072, true>(b, l16, rws, soh); wait_vm<0>(a, b);
        if (!empty2(a, b)) break;
      }
    }
    {
      constexpr int LOOK = 14;
      v4u win[LOOK];
      f16v acc2[2];
      const u32 wb0 = fresh(kh * 3072 + trbase(lane)), wb1 = fresh(wb0 + 60 * KB), wb2 = fresh(wb0 + 120 * KB), wc0 = fresh(wb0 ^ 1024), wc1 = fresh(wb1 ^ 1024), wc2 = fresh(wb2 ^ 1024);
      u32 pb[7]; sfor<7>([&](auto j) { const int jj = j + rot4; pb[j] = soh + (jj >= 7 ? jj - 7 : jj) * 8192; });
      const u32 l16b = l16 + 4096;
      auto issue = [&](auto i, v4u& d) { constexpr int k = w2_kstep(i); bld16<(k & 3) * 1024, true>(d, (k & 4) ? l16b : l16, rws, pb[k >> 3]); };
      auto addr = [&](auto i) { constexpr int o = w2_pos(i / 2) * 4096; constexpr bool c = i & 1;
                                return o < 60 * KB ? (c ? wc0 : wb0) + o : o < 120 * KB ? (c ? wc1 : wb1) + (o - 60 * KB) : (c ? wc2 : wb2) + (o - 120 * KB); };
      if (L == 0) {
        WAIT_VM(0); BARRIER();
        if (w == 0) { publish(8); await(8, 0, 15); }
        BARRIER();
      }
      sfor<LOOK>([&](auto i) { issue(i, win[i]); });
      res_init(acc2, true);
      if (L == 1) BARRIER();
      gemm_steps<2, 56, LOOK, LOOK, false, 0, 18>(acc2, win, issue, addr RETRY_PASS);
      BARRIER();
      gemm_steps<2, 56, LOOK, LOOK, false, 18, 38>(acc2, win, issue, addr RETRY_PASS);
      BARRIER();
      gemm_steps<2, 56, LOOK, LOOK, false, 38, 52>(acc2, win, issue, addr RETRY_PASS);
      if (L == 0 && w == 1) publish(7);
      gemm_steps<2, 56, LOOK, LOOK, false, 52, 56>(acc2, win, issue, addr RETRY_PASS);
      res_epilogue(acc2, 1, 7, L == 1);
    }
    if (L == 1) break;
    {
      constexpr int LOOK = 12;
      const u32 so = WS_XF + 1 * 2 * MB + T13 * 32 * 1024, wb0 = fresh(L_WQ + trbase(lane)), wb1 = fresh(wb0 + 48 * KB);
      auto issue = [&](auto i, v4u& d) { bld16<(i & 3) * 1024, true>(d, l16, rws, so + (((i >> 2) + rot) & 7) * 4096); };
      auto addr = [&](auto i) { constexpr int o = (i / 2) * 3072 + 1024 + (i & 1) * 1024; return o < 48 * KB ? wb0 + o : wb1 + (o - 48 * KB); };
      auto addrq = [&](auto i) { constexpr int o = i * 3072; return o < 48 * KB ? wb0 + o : wb1 + (o - 48 * KB); };
      f16v acc1[2] = {};
      v4u xq[32];
      sfor<LOOK>([&](auto i) { issue(i, xq[i]); });
      gemm_steps<2, 32, LOOK, 32, true, 0, 20>(acc1, xq, issue, addr RETRY_PASS);
      BARRIER();
      Aux ax; aux_read(ax, false);
      gemm_steps<2, 32, LOOK, 32, true, 20, 32>(acc1, xq, issue, addr RETRY_PASS);
      kv_epilogue(1, ax, acc1[0], acc1[1]);
      f16v accq = mfma(aux_col(ax.c[0]), ax.tf, (f16v){});
      gemm_steps<1, 32, 0, 32, false, 0, 20>(&accq, xq, 0, addrq RETRY_PASS);
      kv_flag(1);
      gemm_steps<1, 32, 0, 32, false, 20, 32>(&accq, xq, 0, addrq RETRY_PASS);
      q_epilogue(ax, accq);
    }
  }
  STAMP(); STAMPCLK(1);
  { IDS(); if (w == 0 && m == 0 && lane0 == 0) bst8(tagv, 0, rws, WS_EPOCH + grp * 256); }
}

extern "C" void kernel_launch(void* const* d_in, const int* in_sizes, int n_in, void* d_out, int out_size, void* d_ws, size_t ws_size, hipStream_t stream) {
  Params p;
  p.x = d_in[0]; p.wqkv = d_in[1]; p.wo = d_in[2]; p.w1 = d_in[4]; p.b1 = d_in[5]; p.w2 = d_in[6]; p.b2 = d_in[7]; p.out = d_out; p.ws = (unsigned char*)d_ws;
  (void)hipFuncSetAttribute((const void*)fwd, hipFuncAttributeMaxDynamicSharedMemorySize, LDS_BYTES);
  hipLaunchKernelGGL(fwd, dim3(256), dim3(512), LDS_BYTES, stream, p);
}
```

```cpp
#include <hip/hip_runtime.h>
#include <type_traits>
#include <utility>

#define WARM 4
#define STAMPS 0

#define GAS __attribute__((address_space(1)))
#define LAS __attribute__((address_space(3)))
#define DI __device__ __forceinline__
typedef unsigned u32;
typedef unsigned long long u64;
typedef u32 v4u __attribute__((ext_vector_type(4)));
typedef u32 v2u __attribute__((ext_vector_type(2)));
typedef int v4i __attribute__((ext_vector_type(4)));
typedef short s4 __attribute__((ext_vector_type(4)));
typedef __bf16 bf8 __attribute__((ext_vector_type(8)));
typedef __bf16 bf2 __attribute__((ext_vector_type(2)));
typedef float f2 __attribute__((ext_vector_type(2)));
typedef float f4 __attribute__((ext_vector_type(4)));
typedef float f16v __attribute__((ext_vector_type(16)));
template <int I> using ic = std::integral_constant<int, I>;

template <class F, int... I> DI void sfor_impl(F&& f, std::integer_sequence<int, I...>) { (f(ic<I>{}), ...); }
template <int N, class F> DI void sfor(F&& f) { sfor_impl(f, std::make_integer_sequence<int, N>{}); }

constexpr int KB = 1024, MB = 1024 * 1024;
constexpr int WS_EPOCH = 0;
constexpr int WS_ST0 = 8 * KB;
constexpr int WS_FLAG = 32 * KB;
constexpr int WS_XF = 1 * MB;
constexpr int WS_ST = WS_XF + 6 * MB;
constexpr int WS_KV = WS_ST + 1 * MB;
constexpr int WS_OF = WS_KV + 8 * MB;
constexpr int WS_HF = WS_OF + 4 * MB;
constexpr int WS_STAMP = WS_HF + 14 * MB;
constexpr int WS_END = WS_STAMP + (STAMPS ? MB : 0);
constexpr int L_KV = 0, L_ONES = 48 * KB, L_TA = 54 * KB, L_TB = 58 * KB, L_WQ = 60 * KB, L_WO = 92 * KB, L_W1 = 0, L_XCH = 44 * KB;
constexpr int L_SD = 156 * KB, L_RS = L_SD + 1024, L_CSQ = L_RS + 512, L_CS1 = L_CSQ + 384, L_B1 = L_CS1 + 512, L_ZERO = L_B1 + 512, L_FLG = L_ZERO + 16, LDS_BYTES = 160 * KB;

struct Params { const void *x, *wqkv, *wo, *w1, *b1, *w2, *b2; void* out; unsigned char* ws; };

DI v4i mk_rsrc(const void* p, u32 bytes) { v4i r; r.x = (int)(u32)(u64)p; r.y = (int)((u32)((u64)p >> 32) & 0xffffu); r.z = (int)bytes; r.w = 0x00020000; return r; }
template <int OFF, bool SC1> DI void bld16(v4u& d, u32 voff, v4i rs, u32 soff) {
  if constexpr (SC1) asm volatile("buffer_load_dwordx4 %0, %1, %2, %3 offen offset:%4 sc1" : "=v"(d) : "v"(voff), "s"(rs), "s"(soff), "i"(OFF));
  else asm volatile("buffer_load_dwordx4 %0, %1, %2, %3 offen offset:%4" : "=v"(d) : "v"(voff), "s"(rs), "s"(soff), "i"(OFF));
}
template <int OFF, bool SC1> DI void bld8(v2u& d, u32 voff, v4i rs, u32 soff) {
  if constexpr (SC1) asm volatile("buffer_load_dwordx2 %0, %1, %2, %3 offen offset:%4 sc1" : "=v"(d) : "v"(voff), "s"(rs), "s"(soff), "i"(OFF));
  else asm volatile("buffer_load_dwordx2 %0, %1, %2, %3 offen offset:%4" : "=v"(d) : "v"(voff), "s"(rs), "s"(soff), "i"(OFF));
}
template <int OFF> DI void bst16(v4u d, u32 voff, v4i rs, u32 soff) {
  asm volatile("buffer_store_dwordx4 %0, %1, %2, %3 offen offset:%4 sc1\n\ts_nop 1" ::"v"(d), "v"(voff), "s"(rs), "s"(soff), "i"(OFF));
}
DI void bst8(v2u d, u32 voff, v4i rs, u32 soff) { asm volatile("buffer_store_dwordx2 %0, %1, %2, %3 offen sc1" ::"v"(d), "v"(voff), "s"(rs), "s"(soff)); }
DI void touch(u32& d, u32 voff, v4i rs, u32 soff) { asm volatile("buffer_load_dword %0, %1, %2, %3 offen" : "=v"(d) : "v"(voff), "s"(rs), "s"(soff)); }
template <bool SC1 = false> DI void dma16(u32 lds, u32 voff, v4i rs, u32 soff) {
  if constexpr (SC1) asm volatile("s_mov_b32 m0, %0\n\ts_nop 0\n\tbuffer_load_dwordx4 %1, %2, %3 offen sc1 lds" ::"s"(lds), "v"(voff), "s"(rs), "s"(soff));
  else asm volatile("s_mov_b32 m0, %0\n\ts_nop 0\n\tbuffer_load_dwordx4 %1, %2, %3 offen lds" ::"s"(lds), "v"(voff), "s"(rs), "s"(soff));
}
template <int N, class T> DI void wait_vm(T& x) { asm volatile("s_waitcnt vmcnt(%1)" : "+v"(x) : "i"(N)); }
template <int N, class T> DI void wait_vm(T& x, T& y) { asm volatile("s_waitcnt vmcnt(%2)" : "+v"(x), "+v"(y) : "i"(N)); }
template <class T> DI void pin(T& x) { asm volatile("" : "+v"(x)); }
#define WAIT_VM(n) asm volatile("s_waitcnt vmcnt(" #n ")" ::: "memory")
#define BARRIER() do { STAMP(); asm volatile("s_waitcnt lgkmcnt(0)\n\ts_barrier" ::: "memory"); } while (0)
#define SBAR() __builtin_amdgcn_sched_barrier(0)
DI f16v mfma(v4u a, v4u b, f16v c) { return __builtin_amdgcn_mfma_f32_32x32x16_bf16(__builtin_bit_cast(bf8, a), __builtin_bit_cast(bf8, b), c, 0, 0, 0); }
DI u32 pk(float lo, float hi) { bf2 r = __builtin_convertvector((f2){lo, hi}, bf2); return __builtin_bit_cast(u32, r); }
DI float blo(u32 v) { return __builtin_bit_cast(float, v << 16); }
DI float bhi(u32 v) { return __builtin_bit_cast(float, v & 0xffff0000u); }
template <class T> DI T ldsr(u32 a) { return *(LAS T*)(size_t)a; }
template <class T> DI void ldsw(u32 a, T v) { *(LAS T*)(size_t)a = v; }
DI v4u trfrag(u32 a) {
  const v2u lo = __builtin_bit_cast(v2u, __builtin_amdgcn_ds_read_tr16_b64_v4i16((LAS s4*)(size_t)a));
  const v2u hi = __builtin_bit_cast(v2u, __builtin_amdgcn_ds_read_tr16_b64_v4i16((LAS s4*)(size_t)(a + 512)));
  return (v4u){lo.x, lo.y, hi.x, hi.y};
}
DI f2 halves(float v) {
  auto r = __builtin_amdgcn_permlane32_swap(__builtin_bit_cast(u32, v), __builtin_bit_cast(u32, v), false, false);
  const u32 a = r[0], b = r[1]; return (f2){__builtin_bit_cast(float, a), __builtin_bit_cast(float, b)};
}
DI float bf(u32 v) { return __builtin_bit_cast(float, v); }
DI u32 fb(float v) { return __builtin_bit_cast(u32, v); }
DI float fadd(float a, float b) { float r; asm("v_add_f32 %0, %1, %2" : "=v"(r) : "v"(a), "v"(b)); return r; }
DI float hsum(float v) { const f2 r = halves(v); return r.x + r.y; }
DI float hmax(float v) { const f2 r = halves(v); return __builtin_fmaxf(r.x, r.y); }
DI float mx3(float a, float b, float c) { return __builtin_fmaxf(__builtin_fmaxf(a, b), c); }
DI float max16(const f16v& s, float m) {
  float a = mx3(m, s[0], s[1]), b = mx3(s[2], s[3], s[4]);
  a = mx3(a, s[5], s[6]); b = mx3(b, s[7], s[8]); a = mx3(a, s[9], s[10]); b = mx3(b, s[11], s[12]); a = mx3(a, s[13], s[14]);
  return mx3(a, b, s[15]);
}
DI v4u pk8(const f16v& v, int o) { return (v4u){pk(v[o], v[o + 1]), pk(v[o + 2], v[o + 3]), pk(v[o + 4], v[o + 5]), pk(v[o + 6], v[o + 7])}; }
#define SPIN_LOG 12
constexpr u32 SPIN_CAP = 1u << SPIN_LOG;
DI u32 fresh(u32 v) { asm volatile("" : "+v"(v)); return v; }
DI u32 sfresh(u32 v) { asm volatile("" : "+s"(v)); return v; }
constexpr u32 EMPTY = 0xffffffffu;
DI u32 umax3(u32 a, u32 b, u32 c) { return max(max(a, b), c); }
DI u32 canon(u32 v) { return min(v, EMPTY - 1); }
DI v4u canon(v4u v) { return (v4u){canon(v.x), canon(v.y), canon(v.z), canon(v.w)}; }
DI bool empty2(const v4u& a, const v4u& b) { return __any(max(umax3(a.x, a.y, a.z), umax3(umax3(a.w, b.x, b.y), b.z, b.w)) == EMPTY); }

#define RETRY_ARG
#define RETRY_PASS
#define RETRY_INC()
template <int P, int N, int NX> DI u32 validate1(v4u (&x)[NX]) {
  v4u &a = x[P % NX], &b = x[(P + 1) % NX];
  wait_vm<N>(a, b);
  return umax3(umax3(a.x, a.y, a.z), a.w, b.x);
}
template <int P, int NX, class FI> DI void validate2(u32 v, v4u (&x)[NX], FI&& issue RETRY_ARG) {
  v4u &a = x[P % NX], &b = x[(P + 1) % NX];
  if (__builtin_expect(__any(max(umax3(v, b.y, b.z), b.w) == EMPTY), 0)) {
#pragma unroll 1
    for (u32 spins = 0; spins < SPIN_CAP; ++spins) {
      __builtin_amdgcn_s_sleep(4); RETRY_INC();
      issue(ic<P>{}, a); issue(ic<P + 1>{}, b);
      wait_vm<0>(a, b);
      if (!empty2(a, b)) break;
    }
  }
}
template <int N> DI void wait_lgkm() { __builtin_amdgcn_s_waitcnt(0xc07f | (N << 8)); }
constexpr int cmin(int a, int b) { return a < b ? a : b; }
constexpr int issued(int s, int KS, int LOOK, int INIT) { return s == 0 ? INIT : cmin(cmin(INIT + 2 * s, ((s - 1) & ~1) + LOOK), KS); }
template <int NT, int KS, int LOOK, int NX, bool SWAPLAST, int S0, int S1, int INIT = LOOK, class FI, class FA>
DI void gemm_steps(f16v* acc, v4u (&x)[NX], FI&& issue, FA&& addr RETRY_ARG) {
  constexpr int DEP = 5, N0 = S0 * NT, N1 = S1 * NT;
  v4u ring[DEP + 1];
  u32 vv;
  sfor<DEP>([&](auto i) { ring[(N0 + i) % (DEP + 1)] = trfrag(addr(ic<N0 + i>{})); });
  SBAR();
  if constexpr (LOOK > 0 && S0 == 0) { vv = validate1<0, INIT - 2>(x); validate2<0>(vv, x, issue RETRY_PASS); }
  sfor<N1 - N0>([&](auto i_) {
    constexpr int i = N0 + i_, s = i / NT, t = i % NT;
    if constexpr ((i_ & 1) == 0 && i + DEP <= N1) { wait_lgkm<2 * DEP - 4>(); SBAR(); }
    if constexpr (SWAPLAST && t == NT - 1) acc[t] = mfma(x[s % NX], ring[i % (DEP + 1)], acc[t]);
    else acc[t] = mfma(ring[i % (DEP + 1)], x[s % NX], acc[t]);
    SBAR();
    if constexpr (i + DEP < N1) ring[(i + DEP) % (DEP + 1)] = trfrag(addr(ic<i + DEP>{}));
    if constexpr (LOOK > 0) {
      constexpr int f0 = issued(s, KS, LOOK, INIT), f1 = issued(s + 1, KS, LOOK, INIT);
      static_assert(NT > 2 || (s & 1) == 0 || f1 == f0, "two-tile loops issue in even steps only");
      if constexpr (t < 2 && f0 + t < f1) { SBAR(); issue(ic<f0 + t>{}, x[(f0 + t) % NX]); }
      if constexpr ((s & 1) == 1 && t == NT - 2 && s + 1 < KS) { SBAR(); vv = validate1<s + 1, f1 - (s + 3)>(x); }
      if constexpr ((s & 1) == 1 && t == NT - 1 && s + 1 < KS) { SBAR(); validate2<s + 1>(vv, x, issue RETRY_PASS); }
    }
    SBAR();
  });
}

#define STAMP() do {} while (0)
#define STAMPCLK(i) do {} while (0)
DI f2 ln_stats(float s1, float s2) {
  const float mean = s1 * (1.0f / 512.0f), var = __builtin_fmaxf(s2 * (1.0f / 512.0f) - mean * mean, 0.0f);
  return (f2){-mean, __builtin_amdgcn_sqrtf(var + 1e-5f)};
}
DI u32 trbase(u32 lane) { return (4 * (lane >> 5) + ((lane & 15) >> 2)) * 64 + ((lane >> 4) & 1) * 32 + (lane & 3) * 8; }
DI float ex2(float x) { return __builtin_amdgcn_exp2f(x); }
DI v4u expk8(const f16v& s, int o) { return (v4u){pk(ex2(s[o]), ex2(s[o + 1])), pk(ex2(s[o + 2]), ex2(s[o + 3])), pk(ex2(s[o + 4]), ex2(s[o + 5])), pk(ex2(s[o + 6]), ex2(s[o + 7]))}; }
DI v4u aux_tok(f2 st, u32 h) {
  const float v = h ? st.y : st.x; const u32 a = pk(v, v) & 0xffffu; const float r = v - blo(a); const u32 b = pk(r, r) & 0xffffu; const float r2 = r - blo(b);
  return (v4u){a | (b << 16), h ? pk(r2, 0.0f) : a, 0u, 0u};
}
DI v4u aux_col(u32 v) { return (v4u){(v & 0xffffu) | (v << 16), v >> 16, 0u, 0u}; }

struct Attn {
  v4u q[2], kf, v0, v1, p0, p1, t0, t1; f16v o[2], nm[2], sc[2]; u32 ka, va, vb, vstr, l16;
  DI void init(u32 lane, int qb) {
    l16 = lane * 16;
    const bool real = (lane & 16) == 0;
    ka = L_KV + qb * 4096 + l16; vstr = real ? 4096u : 512u;
    va = real ? L_KV + qb * 4096 + 2048 + l16 : L_ONES + qb * 512 + (lane & 15) * 16; vb = real ? va + 1024 : va;
  }
  DI f16v addmask() { f16v c; sfor<4>([&](auto g) { const f4 t = ldsr<f4>(L_TA + g * 1024 + l16); sfor<4>([&](auto i) { c[4 * g + i] = t[(int)i]; }); }); return c; }
  template <int D, int MASK, bool QK, bool KN, int PV> DI void stage(u32 a0, u32 a1, u32 kn) {
    const f16v& si = sc[D];
    float e[16];
    sfor<4>([&](auto i) { e[i] = ex2(si[(int)i]); });
    SBAR();
    wait_lgkm<0>();
    if constexpr (QK) sc[1 - D] = mfma(kf, q[1 - D], nm[1 - D]);
    SBAR();
    if constexpr (KN) kf = ldsr<v4u>(kn);
    sfor<4>([&](auto i) { e[4 + i] = ex2(si[4 + i]); });
    SBAR();
    if constexpr (PV == 1) o[1 - D] = mfma(v0, p0, o[1 - D]);
    if constexpr (PV == 2) o[1 - D] = mfma(v0, p0, (f16v){});
    SBAR();
    v0 = ldsr<v4u>(a0);
    sfor<4>([&](auto i) { e[8 + i] = ex2(si[8 + i]); });
    SBAR();
    if constexpr (PV) o[1 - D] = mfma(v1, p1, o[1 - D]);
    SBAR();
    v1 = ldsr<v4u>(a1);
    sfor<4>([&](auto i) { e[12 + i] = ex2(si[12 + i]); });
    SBAR();
    sfor<4>([&](auto i) { p0[(int)i] = pk(e[2 * i], e[2 * i + 1]); });
    sfor<4>([&](auto i) { p1[(int)i] = pk(e[8 + 2 * i], e[9 + 2 * i]); });
    if constexpr (MASK == 1) { pin(t0); pin(t1); p0 &= ~t0; p1 &= ~t1; }
    if constexpr (MASK == 2) { p0 &= t0; p1 &= t1; }
    pin(p0); pin(p1);
    SBAR();
  }
  template <class F> DI void diag(F&& mid) {
    const f16v c = addmask();
    v4u kd[2];
    sfor<2>([&](auto d) { kd[d] = ldsr<v4u>(ka + 8 * 4096 + d * 1024); });
    t0 = ldsr<v4u>(L_TB + l16); t1 = ldsr<v4u>(L_TB + 1024 + l16);
    SBAR();
    sfor<2>([&](auto d) { sc[d] = mfma(kd[d], q[d], c); });
    va = fresh(va + 8 * vstr); vb = fresh(vb + 8 * vstr);
    SBAR();
    sfor<2>([&](auto d) {
      const float mx = hmax(max16(sc[d], sc[d][0]));
      sfor<16>([&](auto r) { nm[d][(int)r] = -mx; });
      pin(nm[d]);
      sc[d] = mfma(kd[d], q[d], nm[d]);
      SBAR();
    });
    stage<0, 1, false, false, 0>(va, vb, 0);
    mid();
    stage<1, 1, false, false, 2>(va + 256, vb + 256, 0);
  }
  template <class F, class G> DI void rest(G&& mid, F&& hint) {
    sc[0] = mfma(ldsr<v4u>(ka + 7 * 4096), q[0], nm[0]); kf = ldsr<v4u>(ka + 7 * 4096 + 1024);
    sfor<8>([&](auto i) {
      constexpr int j = 7 - i;
      va = fresh(va - vstr); vb = fresh(vb - vstr);
      if constexpr (j == 3) mid();
      if constexpr (j == 1) hint();
      stage<0, j == 0 ? 2 : 0, true, (j > 0), j == 7 ? 2 : 1>(va, vb, ka + (j - 1) * 4096);
      stage<1, j == 0 ? 2 : 0, (j > 0), (j > 0), 1>(va + 256, vb + 256, ka + (j - 1) * 4096 + 1024);
    });
    o[1] = mfma(v0, p0, o[1]); o[1] = mfma(v1, p1, o[1]);
  }
  template <int D> DI v4u finish(int jmin) {
    f16v& oo = o[D];
    float big = __builtin_fabsf(oo[0]); sfor<7>([&](auto r) { big += __builtin_fabsf(oo[r + 1]); });
    if (__builtin_expect(!__all(oo[8] <= 0x1p60f && big <= 0x1p126f), 0)) {
      auto scores = [&](int j) {
        f16v c = addmask();
        if (j != 8) sfor<16>([&](auto r) { c[(int)r] = (j == 0 && c[(int)r] == 0.0f) ? -__builtin_inff() : 0.0f; });
        return mfma(ldsr<v4u>(ka + j * 4096 + D * 1024), q[D], c);
      };
      float mx = -__builtin_inff();
#pragma unroll 1
      for (int j = jmin; j <= 8; ++j) mx = max16(scores(j), mx);
      mx = hmax(mx);
      oo = (f16v){};
#pragma unroll 1
      for (int j = jmin; j <= 8; ++j) {
        const f16v s = scores(j) - mx;
        oo = mfma(ldsr<v4u>(va + j * vstr + D * 256), expk8(s, 0), oo); oo = mfma(ldsr<v4u>(vb + j * vstr + D * 256), expk8(s, 8), oo);
      }
    }
    const float rl = __builtin_amdgcn_rcpf(oo[8]);
    sfor<8>([&](auto r) { oo[(int)r] *= rl; });
    return canon(pk8(oo, 0));
  }
};

constexpr int w2_kstep(int i) { return ((i % 14) / 2) * 8 + 2 * (i / 14) + (i & 1); }
constexpr int w2r_slot(int i) { return i < 6 ? 6 + 2 * i : 38 + 2 * (i - 6); }
constexpr int w2_pos(int i) { return i < 38 ? (i + 32) % 38 : (i - 6) % 39; }

__global__ void __launch_bounds__(512, 2) fwd(Params p) {
  extern __shared__ __attribute__((aligned(16))) unsigned char smem[];
  u64 pc0; asm volatile("s_getpc_b64 %0" : "=s"(pc0));
  const int w0 = __builtin_amdgcn_readfirstlane(threadIdx.x >> 6);
#define IDS() const int w = sfresh(w0), bid = sfresh(blockIdx.x), grp = (bid & 7) * 2 + (bid >> 7), m = (bid >> 3) & 15, cu = grp * 16 + m, hw = w & 3, cb = m & 7, th = m >> 3, \
              rt = w & 1, kh = (w >> 1) & 1, T13 = 4 * grp + hw, T24 = 4 * grp + 2 * th + rt, rot = 4 * (m & 1) + 2 * (grp & 1) + ((m >> 1) & 1), rot2 = (th + 2 * grp + cb) & 3, rot4 = (cb + 2 * th + 4 * (grp & 1)) % 7; (void)rot; (void)rot2; (void)rot4; (void)cu; (void)cb; (void)kh; (void)T13; (void)T24
  const v4i rws = mk_rsrc(p.ws, WS_END);
  const u32 lane0 = threadIdx.x & 63;
  STAMP(); STAMPCLK(0);
  v2u tagv;
  auto load_tags = [&]() { IDS(); const u32 g = grp + (lane0 < 3 ? lane0 : 0); bld8<0, true>(tagv, (g < 16 ? g : grp) * 256, rws, WS_EPOCH); };
  auto fix_tags = [&]() { pin(tagv); tagv = __builtin_bit_cast(v2u, __builtin_bit_cast(u64, tagv) + 1); };
  auto tag0 = [&]() { return (v2u){(u32)__builtin_amdgcn_readfirstlane(tagv.x), (u32)__builtin_amdgcn_readfirstlane(tagv.y)}; };
  auto publish = [&](int sid) { IDS(); const u32 lane = fresh(lane0); if (lane < 16) bst8(tag0(), lane * 128 + m * 8, rws, WS_FLAG + (sid * 256 + grp * 16) * 128); };
  auto await = [&](int sid, u32 first, u32 mask) {
    IDS(); const u64 t0 = __builtin_bit_cast(u64, tag0()); const u32 vo = (fresh(lane0) & mask) * 8;
#pragma unroll 1
    for (u32 spins = 0; spins < SPIN_CAP; ++spins) {
      v2u f; bld8<0, true>(f, vo, rws, WS_FLAG + (sid * 256 + cu) * 128 + first * 8); wait_vm<0>(f);
      if (__all(__builtin_bit_cast(u64, f) == t0)) break;
      __builtin_amdgcn_s_sleep(2);
    }
  };
  const u32 wrow = lane0 >> 2, wch = (lane0 & 3) * 16;
  if (w0 >= 4) {
    const v4i ro = mk_rsrc(p.wo, 512 * KB), r1 = mk_rsrc(p.w1, 1792 * KB), r2 = mk_rsrc(p.w2, 1792 * KB);
    u32 vo24, vo1;
    { IDS(); vo24 = wrow * 1024 + wch + cb * 128; vo1 = wrow * 3584 + wch + m * 256; }
    auto stage_w1 = [&](int s0, int s1) {
      IDS();
      if (m >= 14) return;
#pragma unroll 1
      for (int s = s0 + hw; s < s1; s += 4) {
        const u32 so = ((s + 17 + 4 * rot) & 31) * 16 * 3584;
        dma16(L_W1 + s * 4096, vo1, r1, so); dma16(L_W1 + s * 4096 + 1024, vo1, r1, so + 64); dma16(L_W1 + s * 4096 + 2048, vo1, r1, so + 128); dma16(L_W1 + s * 4096 + 3072, vo1, r1, so + 192);
      }
    };
    auto w2_row = [&](int i, int rot4) { const int t = (i * 37) >> 9, r = i - 14 * t, j = (r >> 1) + rot4; return (u32)(((j >= 7 ? j - 7 : j) * 8 + 2 * t + (r & 1)) * 16384); };
    auto stage_w2 = [&](int i0, int i1, int p0) {
      IDS();
#pragma unroll 1
      for (int i = i0 + hw; i < i1; i += 4) {
        const u32 la = (i + p0) * 4096, so = w2_row(i, rot4);
        dma16(la, vo24, r2, so); dma16(la + 1024, vo24, r2, so + 64); dma16(la + 2048, vo24, r2, so + 56 * 16384); dma16(la + 3072, vo24, r2, so + 56 * 16384 + 64);
      }
    };
    const v4u ones = {0x3f803f80u, 0x3f803f80u, 0x3f803f80u, 0x3f803f80u}, empt = {EMPTY, EMPTY, EMPTY, EMPTY};
    auto tables = [&]() {
      IDS(); const u32 lane = fresh(lane0), l16 = lane * 16; const int dq = (int)(lane & 31) - 4 * (int)(lane >> 5);
      sfor<4>([&](auto g) { f4 t; sfor<4>([&](auto i) { t[(int)i] = 8 * g + i <= dq ? 0.0f : -__builtin_inff(); }); ldsw<f4>(L_TA + g * 1024 + l16, t); });
      sfor<2>([&](auto g) { v4u b; sfor<4>([&](auto d) { const int c = 16 * g + 8 * (d >> 1) + 2 * (d & 1); b[(int)d] = (c > dq ? 0xffffu : 0u) | (c + 1 > dq ? 0xffff0000u : 0u); });
                            ldsw<v4u>(L_TB + g * 1024 + l16, b); });
      sfor<6>([&](auto g) { ldsw<v4u>(L_ONES + g * 1024 + l16, grp + g / 2 >= 2 ? ones : (v4u){}); });
    };
    auto cs_write = [&](const f16v& a, u32 dst) {
      const u32 lane = fresh(lane0);
      if ((lane & 31) == 0) sfor<4>([&](auto g) { v4u q; sfor<4>([&](auto i) { const float v = a[4 * g + i]; const u32 hi = pk(v, v) & 0xffffu; q[(int)i] = hi | (pk(0.0f, v - blo(hi)) & 0xffff0000u); });
                                                  ldsw<v4u>(dst + (8 * g + 4 * (lane >> 5)) * 4, q); });
    };
    auto put_stats = [&](f2 a, f2 b, u32 tok2) { ldsw<f4>(L_SD + tok2 * 16, (f4){a.x, a.y, b.x, b.y}); ldsw<f2>(L_RS + tok2 * 8, (f2){__builtin_amdgcn_rcpf(a.y), __builtin_amdgcn_rcpf(b.y)}); };
    auto fill_h = [&](int L) {
      IDS();
      if (m < 14) { const u32 so = WS_HF + L * 7 * MB + (T13 * 112 + m * 8) * 1024, l16 = fresh(lane0) * 16;
#pragma unroll 1
                    for (int i = 0; i < 8; ++i) { bst16<0>(empt, l16, rws, so + i * 1024); WAIT_VM(3); } }
    };
    v4u keep[24], w2r[30];
    {
      IDS();
      const u32 l16 = lane0 * 16;
      if (hw < 3) {
        const v4i rq = mk_rsrc(p.wqkv, 1536 * KB);
        const u32 vo = (8 * ((wrow >> 2) & 1) + 4 * (wrow >> 3) + (wrow & 3)) * 3072 + wch + m * 64 + hw * 1024;
        auto chunk = [&](int c) { const u32 so = 64 * ((c + rot) & 7) * 3072, la = L_WQ + c * 12288 + hw * 1024; sfor<4>([&](auto i) { dma16(la + i * 3072, vo, rq, so + i * 16 * 3072); }); };
        chunk(0); chunk(1);
        BARRIER();
        f16v a = {}; const u32 b0 = L_WQ + hw * 1024 + trbase(lane0);
        auto landed = [&](int c) {
          ldsw<u32>(L_FLG + hw * 4, c + 1);
          v4u q[4]; sfor<4>([&](auto i) { q[i] = trfrag(b0 + (4 * c + i) * 3072); }); SBAR();
          sfor<4>([&](auto i) { a = mfma(q[i], ones, a); });
        };
        WAIT_VM(4); ldsw<u32>(L_FLG + hw * 4, 1);
        chunk(2);
        landed(0);
#pragma unroll 1
        for (int c = 1; c < 6; ++c) { chunk(c + 2); WAIT_VM(8); landed(c); }
        WAIT_VM(4); landed(6);
        load_tags();
        WAIT_VM(1); landed(7);
        cs_write(a, L_CSQ + hw * 128);
        WAIT_VM(0); fix_tags();
      } else {
        v4u xr[8];
        const v4i rx = mk_rsrc(p.x, 2 * MB); const u32 vx = (lane0 >> 3) * 1024 + (lane0 & 7) * 16, so = (grp * 128 + m * 8) * 1024;
        sfor<8>([&](auto i) { bld16<i * 128, false>(xr[i], vx, rx, so); });
        if (lane0 < 4) ldsw<u32>(L_FLG + lane0 * 4, lane0 == 3 ? 255u : 0u);
        BARRIER();
        load_tags();
        u32 dd[6] = {};
        {
          touch(dd[3], lane0 < 56 ? lane0 * 128 : 0x7fffffffu, r1, bid * 7168); touch(dd[4], lane0 < 56 ? lane0 * 128 : 0x7fffffffu, r2, bid * 7168); touch(dd[5], lane0 < 16 ? lane0 * 128 : 0x7fffffffu, ro, bid * 2048);
        }
        tables();
        WAIT_VM(0); fix_tags();
        sfor<6>([&](auto i) { pin(dd[i]); });
        sfor<8>([&](auto i) { pin(xr[i]); });
        float s1 = 0.f, s2 = 0.f;
        sfor<32>([&](auto i) { const u32 eu = xr[i / 4][i % 4]; const bf2 e = __builtin_bit_cast(bf2, eu);
                               s1 = __builtin_amdgcn_fdot2_f32_bf16(e, __builtin_bit_cast(bf2, 0x3f803f80u), s1, false); s2 = __builtin_amdgcn_fdot2_f32_bf16(e, e, s2, false); });
        sfor<3>([&](auto i) { s1 += __shfl_xor(s1, 1 << i); s2 += __shfl_xor(s2, 1 << i); });
        if ((lane0 & 7) == 0) bst8(__builtin_bit_cast(v2u, ln_stats(s1, s2)), lane0, rws, WS_ST0 + grp * 1024 + m * 64);
        sfor<4>([&](auto i) { const u32 sf = WS_OF + ((4 * grp + i) * 32 + 2 * m) * 1024; bst16<0>(empt, l16, rws, sf); bst16<1024>(empt, l16, rws, sf); });
        WAIT_VM(0);
        publish(2);
        __builtin_amdgcn_s_sleep(32);
        await(2, 0, 15);
        v4u q; bld16<0, true>(q, l16, rws, WS_ST0 + grp * 1024); wait_vm<0>(q);
        const f4 s = __builtin_bit_cast(f4, q); put_stats((f2){s[0], s[1]}, (f2){s[2], s[3]}, lane0);
      }
    }
    sfor<2>([&](auto L_) {
      constexpr int L = L_;
      IDS();
      const u32 lane = fresh(lane0), l16 = lane * 16;
      auto gather_stats = [&](int buf) {
        __builtin_amdgcn_s_setprio(3);
        v4u q[4]; const u32 so = WS_ST + buf * 256 * KB + T13 * 4096;
#pragma unroll 1
        for (u32 spins = 0; spins < SPIN_CAP; ++spins) {
          bld16<0, true>(q[0], l16, rws, so); bld16<1024, true>(q[1], l16, rws, so); bld16<2048, true>(q[2], l16, rws, so); bld16<3072, true>(q[3], l16, rws, so);
          wait_vm<0>(q[0], q[1]); wait_vm<0>(q[2], q[3]);
          if (!empty2(q[0], q[1]) && !empty2(q[2], q[3])) break;
          __builtin_amdgcn_s_sleep(4);
        }
        float s[4];
        sfor<4>([&](auto i) { const u32 a = q[0][(int)i], b = q[1][(int)i], c = q[2][(int)i], d = q[3][(int)i];
                              float v = fadd(fadd(bf(a), bf(b)), fadd(bf(c), bf(d)));
                              auto r = __builtin_amdgcn_permlane16_swap(fb(v), fb(v), false, false); const u32 r0 = r[0], r1 = r[1];
                              const f2 hh = halves(fadd(bf(r0), bf(r1))); s[i] = fadd(hh.x, hh.y); });
        if (lane < 16) put_stats(ln_stats(s[0], s[1]), ln_stats(s[2], s[3]), hw * 16 + lane);
        __builtin_amdgcn_s_setprio(0);
      };
      auto fill_x = [&](int buf) { const u32 so = WS_XF + buf * 2 * MB + (T24 * 32 + cb * 4 + kh * 2) * 1024; bst16<0>(empt, l16, rws, so); bst16<1024>(empt, l16, rws, so);
                                   if (lane < 16) bst16<0>(empt, l16, rws, WS_ST + buf * 256 * KB + (T24 * 16 + cb * 2 + kh) * 256); };
      auto stage_wo = [&](int s0, int s1) {
#pragma unroll 1
        for (int s = s0 + hw; s < s1; s += 4) { const u32 so = ((s & 16) + ((s + 4 * rot2) & 15)) * 16384; dma16(L_WO + s * 2048, vo24, ro, so); dma16(L_WO + s * 2048 + 1024, vo24, ro, so + 64); } };
      auto w2_get = [&](v4u* r, int s0) { const u32 so = w2_row(s0, rot4) + (hw & 1) * 16384 + (hw >> 1) * 56 * 16384; bld16<0, false>(r[0], vo24, r2, so); bld16<64, false>(r[1], vo24, r2, so); };
      auto w2_set = [&](v4u* r, int s0, u32 ta) { pin(r[0]); pin(r[1]); const u32 o = ta + ((hw & 1) ? w2_pos(s0 + 1) : w2_pos(s0)) * 4096; ldsw<v4u>(o, r[0]); ldsw<v4u>(o + 1024, r[1]); };
      auto w2_regs = [&](auto i0, auto n) { sfor<n>([&](auto j) { constexpr int i = i0 + j; w2_get(&w2r[2 * i], w2r_slot(i)); }); };
      auto w2_put = [&](auto i0, auto n) { const u32 ta = fresh(l16 + (hw >> 1) * 2048); sfor<n>([&](auto j) { constexpr int i = i0 + j; w2_set(&w2r[2 * i], w2r_slot(i), ta); }); };
      v4u st1[24];
      auto w1_pos = [&](int i) { const int j = hw + 4 * i; return j < 11 ? j : j == 11 ? 23 : j < 16 ? j - 1 : j + 8; };
      auto w1_get = [&](auto i0, auto n) { if (m < 14) sfor<n>([&](auto j) { constexpr int i = i0 + j; const u32 so = ((w1_pos(i) + 17 + 4 * rot) & 31) * 16 * 3584;
                                                                             sfor<4>([&](auto t) { bld16<t * 64, false>(st1[4 * i + t], vo1, r1, so); }); }); };
      auto w1_put = [&](auto i0, auto n) { if (m < 14) sfor<n>([&](auto j) { constexpr int i = i0 + j; const u32 o = fresh(l16) + L_W1 + w1_pos(i) * 4096;
                                                                             sfor<4>([&](auto t) { pin(st1[4 * i + t]); ldsw<v4u>(o + t * 1024, st1[4 * i + t]); }); }); };
      f16v cs = {};
      auto w1_colsum = [&](int p0, int p1) {
        const u32 b0 = L_W1 + hw * 1024 + trbase(lane);
#pragma unroll 1
        for (int z = p0; z < p1; z += 4) { v4u q[4]; sfor<4>([&](auto i) { q[i] = trfrag(b0 + (z + i < p1 ? z + i : p0) * 4096); }); SBAR();
                                           sfor<4>([&](auto i) { cs = mfma(q[i], z + i < p1 ? ones : (v4u){}, cs); }); }
      };
      auto halo = [&]() {
        __builtin_amdgcn_s_setprio(3);
        if (4 * grp + 2 * hw >= 8) {
          await(L, 2 * hw, 1);
          sfor<8>([&](auto i) { dma16<true>(L_KV + hw * 8192 + i * 1024, l16, rws, WS_KV + L * 4 * MB + ((4 * grp - 8 + 2 * hw + (i >> 2)) * 16 + m) * 4096 + (i & 3) * 1024); });
        } else sfor<8>([&](auto i) { ldsw<v4u>(L_KV + hw * 8192 + i * 1024 + l16, (v4u){}); });
        __builtin_amdgcn_s_setprio(0);
      };
      BARRIER();
      if (L == 0) sfor<24>([&](auto i) { keep[i] = ldsr<v4u>(L_WQ + hw * 24 * KB + i * 1024 + l16); });
      else if (hw == 3) tables();
      BARRIER();
      if (L == 0) w1_get(ic<3>{}, ic<2>{});
      halo();
      fill_x(2 * L);
      WAIT_VM(3); BARRIER();
      stage_wo(0, 32);
      WAIT_VM(16);
      if (L == 1) w1_get(ic<3>{}, ic<2>{});
      BARRIER();
      stage_w1(15, 23);
      w1_get(ic<0>{}, ic<3>{});
      WAIT_VM(0);
      BARRIER();
      w1_get(ic<5>{}, ic<1>{});
      w1_put(ic<0>{}, ic<2>{});
      if (hw < 3) w1_put(ic<2>{}, ic<1>{});
      BARRIER();
      if (L == 0) {
        if (m < 14) {
          w1_colsum(0, 11); w1_colsum(15, 23);
          if (hw == 0) { const u32 v = ((const GAS u32*)p.b1)[m * 64 + lane]; ldsw<v2u>(L_B1 + lane * 8, (v2u){(v & 0xffffu) | (v << 16), (v >> 16) | (v & 0xffff0000u)}); }
        }
      }
      WAIT_VM(0);
      BARRIER();
      w1_put(ic<4>{}, ic<2>{});
      if (hw == 3) w1_put(ic<2>{}, ic<1>{});
      BARRIER();
      gather_stats(2 * L);
      w1_put(ic<3>{}, ic<1>{});
      if (L == 0 && m < 14) w1_colsum(23, 32);
      BARRIER();
      if (L == 0 && m < 14) { w1_colsum(11, 15); cs_write(cs, L_CS1 + hw * 128); }
      stage_w2(0, 6, 32);
      v4u st2[20];
      if (L == 0) w2_regs(ic<0>{}, ic<6>{}); else sfor<6>([&](auto i) { w2_get(&st2[2 * i], 18 + 2 * i); });
      BARRIER();
      if (L == 0) w2_regs(ic<6>{}, ic<4>{}); else sfor<4>([&](auto i) { w2_get(&st2[12 + 2 * i], 30 + 2 * i); });
      WAIT_VM(8);
      BARRIER();
      w2_put(ic<0>{}, ic<6>{});
      if constexpr (L == 0) {
        stage_w2(18, 26, -6);
        fill_x(1); const u32 so = WS_OF + 2 * MB + (T13 * 32 + 2 * m) * 1024; bst16<0>(empt, l16, rws, so); bst16<1024>(empt, l16, rws, so);
        WAIT_VM(0);
        BARRIER();
        stage_w2(26, 38, -6);
        w2_regs(ic<10>{}, ic<5>{});
        BARRIER();
        WAIT_VM(0);
      } else {
        WAIT_VM(0);
        const u32 ta = fresh(l16 + (hw >> 1) * 2048);
        sfor<10>([&](auto i) { w2_set(&st2[2 * i], 18 + 2 * i, ta); });
        BARRIER();
      }
      BARRIER();
      w2_put(ic<6>{}, ic<9>{});
      BARRIER();
      const u32 ka = fresh(L_WQ + hw * 24 * KB + (8 * ((lane >> 4) & 1) + 4 * (lane >> 5) + ((lane >> 2) & 3)) * 64 + (lane & 3) * 16);
      if (L == 0) { if (hw < 2) sfor<24>([&](auto i) { ldsw<v4u>(ka + i * 1024, keep[i]); }); else fill_h(1); }
      BARRIER();
      if constexpr (L == 0) {
        if (hw >= 2) sfor<24>([&](auto i) { ldsw<v4u>(ka + i * 1024, keep[i]); });
        BARRIER();
        gather_stats(1);
        if (hw < 2) fill_h(1);
      }
    });
    STAMP();
    return;
  }
  v4u res[2], b2p[2];
  v4u qf[2];
  struct Aux { v4u tf; u32 c[4]; float rs, sd; f4 rv[4]; };
  auto aux_read = [&](Aux& x, bool ffn) {
    IDS();
    const u32 lane = fresh(lane0), h = lane >> 5, t32 = lane & 31;
    const f2 st = ldsr<f2>(L_SD + hw * 256 + t32 * 8);
    x.rs = ldsr<float>(L_RS + hw * 128 + t32 * 4);
    if (ffn) { const u32 lc = fresh(L_CS1 + h * 512 + t32 * 4); sfor<4>([&](auto t) { x.c[t] = ldsr<u32>(lc + t * 128); }); }
    else { const u32 lc = fresh(L_CSQ + t32 * 4), lr = fresh(L_RS + hw * 128 + 16 * h);
           sfor<3>([&](auto t) { const u32 v = ldsr<u32>(lc + t * 128); x.c[t] = h ? 0u : v; });
           sfor<4>([&](auto g) { x.rv[g] = ldsr<f4>(lr + 32 * g); }); }
    x.tf = aux_tok(st, h); x.sd = st.y;
  };
  auto kv_epilogue = [&](int L, const Aux& x, f16v& ak, f16v& av) {
    IDS();
    const u32 l16 = fresh(lane0) * 16;
    ak = mfma(aux_col(x.c[1]), x.tf, ak); av = mfma(x.tf, aux_col(x.c[2]), av);
    const u32 so = WS_KV + L * 4 * MB + (T13 * 16 + m) * 4096, lk = fresh(L_KV + (8 + hw) * 4096 + l16);
    ak *= x.rs;
    const v4u k0 = pk8(ak, 0), k1 = pk8(ak, 8); bst16<0>(k0, l16, rws, so); bst16<1024>(k1, l16, rws, so);
    sfor<16>([&](auto r) { av[(int)r] *= x.rv[r / 4][r % 4]; });
    const v4u v0 = pk8(av, 0), v1 = pk8(av, 8); bst16<2048>(v0, l16, rws, so); bst16<3072>(v1, l16, rws, so);
    ldsw<v4u>(lk, k0); ldsw<v4u>(lk + 1024, k1); ldsw<v4u>(lk + 2048, v0); ldsw<v4u>(lk + 3072, v1);
  };
  auto q_epilogue = [&](const Aux& x, f16v aq) { aq *= x.rs * (0.25f * 1.4426950408889634f); qf[0] = pk8(aq, 0); qf[1] = pk8(aq, 8); };
  auto kv_flag = [&](int L) {
    IDS(); const u32 lane = fresh(lane0);
    WAIT_VM(0);
    if ((lane == 1 || lane == 2) && grp + lane < 16) bst8(tagv, lane * (16 * 128 - 32) + 64 + hw * 8, rws, WS_FLAG + (L * 256 + cu) * 128);
  };
  {
    IDS();
    f16v acc1[3] = {};
    const u32 lane = lane0, h = lane >> 5, t32 = lane & 31;
    const v4i rx = mk_rsrc(p.x, 2 * MB);
    const u32 vo = wrow * 1024 + (((lane & 3) ^ ((lane >> 4) & 3)) * 16), so = T13 * 32 * 1024, xs = hw * 12 * KB;
    auto issue = [&](int u) { const u32 o = u < 16 ? so + ((u + 2 * rot) & 15) * 64 : T24 * 32 * 1024 + cb * 128 + kh * 64;
                              dma16(xs + (u % 6) * 2048, vo, rx, o); dma16(xs + (u % 6) * 2048 + 1024, vo, rx, o + 16384); };
    issue(0); issue(1);
    BARRIER();
    load_tags();
    v2u bb[4];
    { const v4i rb = mk_rsrc(p.b2, 1024); const u32 bo = (cb * 64 + kh * 32 + 4 * h) * 2; sfor<4>([&](auto i) { bld8<i * 16, false>(bb[i], bo, rb, 0); }); }
    issue(2); issue(3);
    const u32 xa0 = xs + t32 * 64 + ((h ^ ((t32 >> 2) & 3)) * 16), xa1 = xa0 ^ 32;
    const u32 wb0 = fresh(L_WQ + trbase(lane)), wb1 = fresh(wb0 + 48 * KB);
    auto addr = [&](auto i) { constexpr int o = i * 1024; return o < 48 * KB ? wb0 + o : wb1 + (o - 48 * KB); };
    auto chunk_wait = [&](u32 c) {
#pragma unroll 1
      for (u32 spins = 0; spins < SPIN_CAP; ++spins) { const v4u f = *(volatile LAS v4u*)(size_t)L_FLG; if ((u32)__builtin_amdgcn_readfirstlane(min(min(f.x, f.y), min(f.z, f.w))) > c) break; }
      asm volatile("" ::: "memory");
    };
    v4u xf[4], ring[5];
    WAIT_VM(11);
    xf[0] = ldsr<v4u>(xa0); xf[1] = ldsr<v4u>(xa1);
    chunk_wait(0);
    sfor<4>([&](auto j) { ring[j] = trfrag(addr(j)); });
    SBAR();
    sfor<96>([&](auto i_) {
      constexpr int i = i_, s = i / 3, t = i % 3, u = s / 2;
      if constexpr (t == 2) acc1[t] = mfma(xf[s % 4], ring[i % 5], acc1[t]); else acc1[t] = mfma(ring[i % 5], xf[s % 4], acc1[t]);
      SBAR();
      if constexpr (i + 4 < 96) { if constexpr ((i + 4) % 12 == 0) chunk_wait((i + 4) / 12); ring[(i + 4) % 5] = trfrag(addr(ic<i + 4>{})); }
      if constexpr (i % 6 == 0 && u + 4 < 17) { SBAR(); issue(u + 4); }
      if constexpr (i % 6 == 1 && u + 1 < 16) {
        asm volatile("s_waitcnt vmcnt(%0)" ::"i"(2 * ((u + 4 < 16 ? u + 4 : 16) - (u + 1))) : "memory");
        xf[(s + 2) % 4] = ldsr<v4u>(xa0 + ((u + 1) % 6) * 2048); xf[(s + 3) % 4] = ldsr<v4u>(xa1 + ((u + 1) % 6) * 2048);
      }
      SBAR();
    });
    WAIT_VM(0);
    fix_tags();
    sfor<4>([&](auto i) { pin(bb[i]); });
    sfor<2>([&](auto f) { const u32 ra = xs + (16 % 6) * 2048 + t32 * 64 + 8 * h, z = ((t32 >> 2) & 3) * 16;
                          const v2u a = ldsr<v2u>(ra + ((2 * f * 16) ^ z)), b = ldsr<v2u>(ra + (((2 * f + 1) * 16) ^ z));
                          res[f] = (v4u){a.x, a.y, b.x, b.y}; b2p[f] = (v4u){bb[2 * f].x, bb[2 * f].y, bb[2 * f + 1].x, bb[2 * f + 1].y}; });
    BARRIER();
    Aux ax; aux_read(ax, false);
    SBAR();
    acc1[0] = mfma(aux_col(ax.c[0]), ax.tf, acc1[0]);
    kv_epilogue(0, ax, acc1[1], acc1[2]);
    q_epilogue(ax, acc1[0]);
  }
  for (int L = 0;; ++L) {
    IDS();
    const u32 lane = fresh(lane0), l16 = lane * 16, h = lane >> 5, t32 = lane & 31;
    auto res_init = [&](f16v (&acc)[2], bool bias) {
      sfor<8>([&](auto i) { const u32 r = res[i / 4][i % 4], b = bias ? fresh(b2p[i / 4][i % 4]) : 0u;
                            acc[0][2 * i] = bias ? blo(r) + blo(b) : blo(r); acc[0][2 * i + 1] = bias ? bhi(r) + bhi(b) : bhi(r); });
      acc[1] = (f16v){};
      pin(acc[0]);
    };
    auto res_epilogue = [&](f16v (&acc)[2], int buf, int sid, bool last) {
      const u32 xa = fresh(L_XCH + w * 4096 + l16), xb = fresh(L_XCH + (w ^ 2) * 4096 + l16);
      const u32 so = sfresh(WS_XF + buf * 2 * MB + (T24 * 32 + cb * 4 + kh * 2) * 1024), ss = sfresh(WS_ST + buf * 256 * KB + (T24 * 16 + cb * 2 + kh) * 256);
      sfor<4>([&](auto g) { ldsw<f4>(xa + g * 1024, (f4){acc[1][4 * g], acc[1][4 * g + 1], acc[1][4 * g + 2], acc[1][4 * g + 3]}); });
      BARRIER();
      f4 o[4]; sfor<4>([&](auto g) { o[g] = ldsr<f4>(xb + g * 1024); });
      f16v v; sfor<16>([&](auto r) { v[(int)r] = acc[0][(int)r] + o[r / 4][r % 4]; });
      res[0] = canon(pk8(v, 0)); res[1] = canon(pk8(v, 8));
      if (!last) {
        bst16<0>(res[0], l16, rws, so); bst16<1024>(res[1], l16, rws, so);
        SBAR();
        float s1 = 0.f, s2 = 0.f;
        sfor<8>([&](auto i) { const u32 eu = res[i / 4][i % 4]; const bf2 e = __builtin_bit_cast(bf2, eu);
                              s1 = __builtin_amdgcn_fdot2_f32_bf16(e, __builtin_bit_cast(bf2, 0x3f803f80u), s1, false); s2 = __builtin_amdgcn_fdot2_f32_bf16(e, e, s2, false); });
        s1 = hsum(s1); s2 = hsum(s2);
        bst8((v2u){canon(fb(s1)), canon(fb(s2))}, t32 * 8, rws, ss);
        if (w == 0) await(sid, 0, 15); else WAIT_VM(0);
        BARRIER();
      } else {
        GAS unsigned char* op = (GAS unsigned char*)p.out + (T24 * 32 + t32) * 1024 + (cb * 64 + kh * 32 + 8 * h) * 2;
        sfor<2>([&](auto f) {
          auto a = __builtin_amdgcn_permlane32_swap(res[f].x, res[f].z, false, false), b = __builtin_amdgcn_permlane32_swap(res[f].y, res[f].w, false, false);
          *(GAS v4u*)(op + f * 32) = (v4u){a[0], b[0], a[1], b[1]}; });
      }
    };
    BARRIER();
    {
      Attn at; at.init(lane, hw); at.q[0] = qf[0]; at.q[1] = qf[1];
      at.diag([&]() { if (L == 0) kv_flag(0); });
      BARRIER();
      at.rest([&]() { BARRIER();   }, [&]() { if (w == 1) publish(3 + L); });
      const u32 so = WS_OF + L * 2 * MB + (T13 * 32 + 2 * m) * 1024; const int jmin = max(8 - T13, 0);
      bst16<0>(at.finish<0>(jmin), l16, rws, so); bst16<1024>(at.finish<1>(jmin), l16, rws, so);
    }
    BARRIER();
    if (w == 0) await(3 + L, 0, 15); else WAIT_VM(0);
    BARRIER();
    {
      constexpr int LOOK = 10;
      v4u win[LOOK];
      f16v acc2[2];
      const u32 so = WS_OF + L * 2 * MB + (T24 * 32 + kh * 16) * 1024, wb = fresh(L_WO + kh * 33 * KB + trbase(lane)), wc = fresh(wb ^ 1024);
      auto issue = [&](auto i, v4u& d) { bld16<(i & 3) * 1024, true>(d, l16, rws, so + (((i >> 2) + rot2) & 3) * 4096); };
      auto addr = [&](auto i) { return ((i & 1) ? wc : wb) + (i / 2) * 2048; };
      sfor<LOOK>([&](auto i) { issue(i, win[i]); });
      res_init(acc2, false);
      gemm_steps<2, 16, LOOK, LOOK, false, 0, 12>(acc2, win, issue, addr RETRY_PASS);
      if (w == 1) publish(5 + L);
      gemm_steps<2, 16, LOOK, LOOK, false, 12, 16>(acc2, win, issue, addr RETRY_PASS);
      res_epilogue(acc2, 2 * L, 5 + L, false);
    }
    const u32 soh = WS_HF + L * 7 * MB + (T24 * 112 + kh * 56) * 1024;
    if (m < 14) {
      constexpr int LOOK = 20;
      v4u win[LOOK];
      f16v acc3[4] = {};
      const u32 so = WS_XF + 2 * L * 2 * MB + T13 * 32 * 1024, wb0 = fresh(L_W1 + trbase(lane)), wb1 = fresh(wb0 + 63 * KB), wb2 = fresh(wb0 + 126 * KB);
      auto issue = [&](auto i, v4u& d) { bld16<(i & 3) * 1024, true>(d, l16, rws, so + (((i >> 2) + rot) & 7) * 4096); };
      auto addr = [&](auto i) { constexpr int o = ((i + 60) & 127) * 1024; return o < 63 * KB ? wb0 + o : o < 126 * KB ? wb1 + (o - 63 * KB) : wb2 + (o - 126 * KB); };
      sfor<8>([&](auto i) { issue(i, win[i]); });
      gemm_steps<4, 32, LOOK, LOOK, false, 0, 14, 8>(acc3, win, issue, addr RETRY_PASS);
      BARRIER();
      gemm_steps<4, 32, LOOK, LOOK, false, 14, 24, 8>(acc3, win, issue, addr RETRY_PASS);
      BARRIER();
      Aux ax; aux_read(ax, true);
      gemm_steps<4, 32, LOOK, LOOK, false, 24, 32, 8>(acc3, win, issue, addr RETRY_PASS);
      sfor<4>([&](auto t) { acc3[t] = mfma(aux_col(ax.c[t]), ax.tf, acc3[t]); });
      BARRIER();
      const float rs = ax.rs, sd = ax.sd;
      const u32 sh = WS_HF + L * 7 * MB + (T13 * 112 + m * 8) * 1024;
      constexpr float K1 = -2.0f * 0.7978845608028654f * 1.4426950408889634f, K2 = K1 * 0.044715f;
      const float c1 = K1 * rs, c2 = K2 * rs * rs * rs;
      sfor<8>([&](auto f) {
        v4u q;
        sfor<4>([&](auto d) {
          const f2 u = {acc3[f / 2][(f & 1) * 8 + 2 * d], acc3[f / 2][(f & 1) * 8 + 2 * d + 1]};
          const f2 z = u * __builtin_elementwise_fma(u * u, (f2){c2, c2}, (f2){c1, c1});
          const f2 dn = __builtin_elementwise_fma((f2){ex2(z.x), ex2(z.y)}, (f2){sd, sd}, (f2){sd, sd});
          const f2 g = u * (f2){__builtin_amdgcn_rcpf(dn.x), __builtin_amdgcn_rcpf(dn.y)};
          q[(int)d] = canon(pk(g.x, g.y));
        });
        bst16<(f & 3) * 1024>(q, l16, rws, sh + (f >> 2) * 4096);
      });
    } else {
      BARRIER(); BARRIER(); BARRIER();
      v4u a, b;
#pragma unroll 1
      for (u32 spins = 0; spins < SPIN_CAP && L == 1; ++spins) {
        __builtin_amdgcn_s_sleep(8);
        bld16<2048, true>(a, l16, rws, soh); bld16<3072, true>(b, l16, rws, soh); wait_vm<0>(a, b);
        if (!empty2(a, b)) break;
      }
    }
    {
      constexpr int LOOK = 14;
      v4u win[LOOK];
      f16v acc2[2];
      const u32 wb0 = fresh(kh * 3072 + trbase(lane)), wb1 = fresh(wb0 + 60 * KB), wb2 = fresh(wb0 + 120 * KB), wc0 = fresh(wb0 ^ 1024), wc1 = fresh(wb1 ^ 1024), wc2 = fresh(wb2 ^ 1024);
      u32 pb[7]; sfor<7>([&](auto j) { const int jj = j + rot4; pb[j] = soh + (jj >= 7 ? jj - 7 : jj) * 8192; });
      const u32 l16b = l16 + 4096;
      auto issue = [&](auto i, v4u& d) { constexpr int k = w2_kstep(i); bld16<(k & 3) * 1024, true>(d, (k & 4) ? l16b : l16, rws, pb[k >> 3]); };
      auto addr = [&](auto i) { constexpr int o = w2_pos(i / 2) * 4096; constexpr bool c = i & 1;
                                return o < 60 * KB ? (c ? wc0 : wb0) + o : o < 120 * KB ? (c ? wc1 : wb1) + (o - 60 * KB) : (c ? wc2 : wb2) + (o - 120 * KB); };
      if (L == 0) {
        WAIT_VM(0); BARRIER();
        if (w == 0) { publish(8); await(8, 0, 15); }
        BARRIER();
      }
      sfor<LOOK>([&](auto i) { issue(i, win[i]); });
      res_init(acc2, true);
      if (L == 1) BARRIER();
      gemm_steps<2, 56, LOOK, LOOK, false, 0, 18>(acc2, win, issue, addr RETRY_PASS);
      BARRIER();
      gemm_steps<2, 56, LOOK, LOOK, false, 18, 38>(acc2, win, issue, addr RETRY_PASS);
      BARRIER();
      gemm_steps<2, 56, LOOK, LOOK, false, 38, 52>(acc2, win, issue, addr RETRY_PASS);
      if (L == 0 && w == 1) publish(7);
      gemm_steps<2, 56, LOOK, LOOK, false, 52, 56>(acc2, win, issue, addr RETRY_PASS);
      res_epilogue(acc2, 1, 7, L == 1);
    }
    if (L == 1) break;
    {
      constexpr int LOOK = 12;
      const u32 so = WS_XF + 1 * 2 * MB + T13 * 32 * 1024, wb0 = fresh(L_WQ + trbase(lane)), wb1 = fresh(wb0 + 48 * KB);
      auto issue = [&](auto i, v4u& d) { bld16<(i & 3) * 1024, true>(d, l16, rws, so + (((i >> 2) + rot) & 7) * 4096); };
      auto addr = [&](auto i) { constexpr int o = (i / 2) * 3072 + 1024 + (i & 1) * 1024; return o < 48 * KB ? wb0 + o : wb1 + (o - 48 * KB); };
      auto addrq = [&](auto i) { constexpr int o = i * 3072; return o < 48 * KB ? wb0 + o : wb1 + (o - 48 * KB); };
      f16v acc1[2] = {};
      v4u xq[32];
      sfor<LOOK>([&](auto i) { issue(i, xq[i]); });
      gemm_steps<2, 32, LOOK, 32, true, 0, 20>(acc1, xq, issue, addr RETRY_PASS);
      BARRIER();
      Aux ax; aux_read(ax, false);
      gemm_steps<2, 32, LOOK, 32, true, 20, 32>(acc1, xq, issue, addr RETRY_PASS);
      kv_epilogue(1, ax, acc1[0], acc1[1]);
      f16v accq = mfma(aux_col(ax.c[0]), ax.tf, (f16v){});
      gemm_steps<1, 32, 0, 32, false, 0, 20>(&accq, xq, 0, addrq RETRY_PASS);
      kv_flag(1);
      gemm_steps<1, 32, 0, 32, false, 20, 32>(&accq, xq, 0, addrq RETRY_PASS);
      q_epilogue(ax, accq);
    }
  }
  STAMP(); STAMPCLK(1);
  { IDS(); if (w == 0 && m == 0 && lane0 == 0) bst8(tagv, 0, rws, WS_EPOCH + grp * 256); }
}

extern "C" void kernel_launch(void* const* d_in, const int* in_sizes, int n_in, void* d_out, int out_size, void* d_ws, size_t ws_size, hipStream_t stream) {
  Params p;
  p.x = d_in[0]; p.wqkv = d_in[1]; p.wo = d_in[2]; p.w1 = d_in[4]; p.b1 = d_in[5]; p.w2 = d_in[6]; p.b2 = d_in[7]; p.out = d_out; p.ws = (unsigned char*)d_ws;
  (void)hipFuncSetAttribute((const void*)fwd, hipFuncAttributeMaxDynamicSharedMemorySize, LDS_BYTES);
  hipLaunchKernelGGL(fwd, dim3(256), dim3(512), LDS_BYTES, stream, p);
}
```

```cpp
#include <hip/hip_runtime.h>
#include <type_traits>
#include <utility>

#define E1 0
#define E2 0
#define E3 0
#define STAMPS 0

#define GAS __attribute__((address_space(1)))
#define LAS __attribute__((address_space(3)))
#define DI __device__ __forceinline__
typedef unsigned u32;
typedef unsigned long long u64;
typedef u32 v4u __attribute__((ext_vector_type(4)));
typedef u32 v2u __attribute__((ext_vector_type(2)));
typedef int v4i __attribute__((ext_vector_type(4)));
typedef short s4 __attribute__((ext_vector_type(4)));
typedef __bf16 bf8 __attribute__((ext_vector_type(8)));
typedef __bf16 bf2 __attribute__((ext_vector_type(2)));
typedef float f2 __attribute__((ext_vector_type(2)));
typedef float f4 __attribute__((ext_vector_type(4)));
typedef float f16v __attribute__((ext_vector_type(16)));
template <int I> using ic = std::integral_constant<int, I>;

template <class F, int... I> DI void sfor_impl(F&& f, std::integer_sequence<int, I...>) { (f(ic<I>{}), ...); }
template <int N, class F> DI void sfor(F&& f) { sfor_impl(f, std::make_integer_sequence<int, N>{}); }

constexpr int KB = 1024, MB = 1024 * 1024;
constexpr int WS_EPOCH = 0;
constexpr int WS_ST0 = 8 * KB;
constexpr int WS_FLAG = 32 * KB;
constexpr int WS_XF = 1 * MB;
constexpr int WS_ST = WS_XF + 6 * MB;
constexpr int WS_KV = WS_ST + 1 * MB;
constexpr int WS_OF = WS_KV + 8 * MB;
constexpr int WS_HF = WS_OF + 4 * MB;
constexpr int WS_STAMP = WS_HF + 14 * MB;
constexpr int WS_END = WS_STAMP + (STAMPS ? MB : 0);
constexpr int L_KV = 0, L_ONES = 48 * KB, L_TA = 54 * KB, L_TB = 58 * KB, L_WQ = 60 * KB, L_WO = 92 * KB, L_W1 = 0, L_XCH = 44 * KB;
constexpr int L_SD = 156 * KB, L_RS = L_SD + 1024, L_CSQ = L_RS + 512, L_CS1 = L_CSQ + 384, L_B1 = L_CS1 + 512, L_ZERO = L_B1 + 512, L_FLG = L_ZERO + 16, LDS_BYTES = 160 * KB;

struct Params { const void *x, *wqkv, *wo, *w1, *b1, *w2, *b2; void* out; unsigned char* ws; };

DI v4i mk_rsrc(const void* p, u32 bytes) { v4i r; r.x = (int)(u32)(u64)p; r.y = (int)((u32)((u64)p >> 32) & 0xffffu); r.z = (int)bytes; r.w = 0x00020000; return r; }
template <int OFF, bool SC1> DI void bld16(v4u& d, u32 voff, v4i rs, u32 soff) {
  if constexpr (SC1) asm volatile("buffer_load_dwordx4 %0, %1, %2, %3 offen offset:%4 sc1" : "=v"(d) : "v"(voff), "s"(rs), "s"(soff), "i"(OFF));
  else asm volatile("buffer_load_dwordx4 %0, %1, %2, %3 offen offset:%4" : "=v"(d) : "v"(voff), "s"(rs), "s"(soff), "i"(OFF));
}
template <int OFF, bool SC1> DI void bld8(v2u& d, u32 voff, v4i rs, u32 soff) {
  if constexpr (SC1) asm volatile("buffer_load_dwordx2 %0, %1, %2, %3 offen offset:%4 sc1" : "=v"(d) : "v"(voff), "s"(rs), "s"(soff), "i"(OFF));
  else asm volatile("buffer_load_dwordx2 %0, %1, %2, %3 offen offset:%4" : "=v"(d) : "v"(voff), "s"(rs), "s"(soff), "i"(OFF));
}
template <int OFF> DI void bst16(v4u d, u32 voff, v4i rs, u32 soff) {
  asm volatile("buffer_store_dwordx4 %0, %1, %2, %3 offen offset:%4 sc1\n\ts_nop 1" ::"v"(d), "v"(voff), "s"(rs), "s"(soff), "i"(OFF));
}
DI void bst8(v2u d, u32 voff, v4i rs, u32 soff) { asm volatile("buffer_store_dwordx2 %0, %1, %2, %3 offen sc1" ::"v"(d), "v"(voff), "s"(rs), "s"(soff)); }
DI void touch(u32& d, u32 voff, v4i rs, u32 soff) { asm volatile("buffer_load_dword %0, %1, %2, %3 offen" : "=v"(d) : "v"(voff), "s"(rs), "s"(soff)); }
template <bool SC1 = false> DI void dma16(u32 lds, u32 voff, v4i rs, u32 soff) {
  if constexpr (SC1) asm volatile("s_mov_b32 m0, %0\n\ts_nop 0\n\tbuffer_load_dwordx4 %1, %2, %3 offen sc1 lds" ::"s"(lds), "v"(voff), "s"(rs), "s"(soff));
  else asm volatile("s_mov_b32 m0, %0\n\ts_nop 0\n\tbuffer_load_dwordx4 %1, %2, %3 offen lds" ::"s"(lds), "v"(voff), "s"(rs), "s"(soff));
}
template <int N, class T> DI void wait_vm(T& x) { asm volatile("s_waitcnt vmcnt(%1)" : "+v"(x) : "i"(N)); }
template <int N, class T> DI void wait_vm(T& x, T& y) { asm volatile("s_waitcnt vmcnt(%2)" : "+v"(x), "+v"(y) : "i"(N)); }
template <class T> DI void pin(T& x) { asm volatile("" : "+v"(x)); }
#define WAIT_VM(n) asm volatile("s_waitcnt vmcnt(" #n ")" ::: "memory")
#define BARRIER() do { SBAR(); if (STAMPS != 3) STAMP(); asm volatile("s_waitcnt lgkmcnt(0)\n\ts_barrier" ::: "memory"); SBAR(); } while (0)
#define STAMPL 0
#define LBAR() do { SBAR(); if (STAMPL) STAMP(); asm volatile("s_barrier" ::: "memory"); SBAR(); } while (0)
#define HBAR() do { if (STAMPL) STAMP(); asm volatile("s_waitcnt lgkmcnt(0)\n\ts_barrier" ::: "memory"); } while (0)
#define SBAR() __builtin_amdgcn_sched_barrier(0)
DI f16v mfma(v4u a, v4u b, f16v c) { return __builtin_amdgcn_mfma_f32_32x32x16_bf16(__builtin_bit_cast(bf8, a), __builtin_bit_cast(bf8, b), c, 0, 0, 0); }
DI u32 pk(float lo, float hi) { bf2 r = __builtin_convertvector((f2){lo, hi}, bf2); return __builtin_bit_cast(u32, r); }
DI float blo(u32 v) { return __builtin_bit_cast(float, v << 16); }
DI float bhi(u32 v) { return __builtin_bit_cast(float, v & 0xffff0000u); }
template <class T> DI T ldsr(u32 a) { return *(LAS T*)(size_t)a; }
template <class T> DI void ldsw(u32 a, T v) { *(LAS T*)(size_t)a = v; }
DI v4u trfrag(u32 a) {
  const v2u lo = __builtin_bit_cast(v2u, __builtin_amdgcn_ds_read_tr16_b64_v4i16((LAS s4*)(size_t)a));
  const v2u hi = __builtin_bit_cast(v2u, __builtin_amdgcn_ds_read_tr16_b64_v4i16((LAS s4*)(size_t)(a + 512)));
  return (v4u){lo.x, lo.y, hi.x, hi.y};
}
DI f2 halves(float v) {
  auto r = __builtin_amdgcn_permlane32_swap(__builtin_bit_cast(u32, v), __builtin_bit_cast(u32, v), false, false);
  const u32 a = r[0], b = r[1]; return (f2){__builtin_bit_cast(float, a), __builtin_bit_cast(float, b)};
}
DI float bf(u32 v) { return __builtin_bit_cast(float, v); }
DI u32 fb(float v) { return __builtin_bit_cast(u32, v); }
DI float fadd(float a, float b) { float r; asm("v_add_f32 %0, %1, %2" : "=v"(r) : "v"(a), "v"(b)); return r; }
DI float hsum(float v) { const f2 r = halves(v); return r.x + r.y; }
DI float hmax(float v) { const f2 r = halves(v); return __builtin_fmaxf(r.x, r.y); }
DI float mx3(float a, float b, float c) { return __builtin_fmaxf(__builtin_fmaxf(a, b), c); }
DI float max16(const f16v& s, float m) {
  float a = mx3(m, s[0], s[1]), b = mx3(s[2], s[3], s[4]);
  a = mx3(a, s[5], s[6]); b = mx3(b, s[7], s[8]); a = mx3(a, s[9], s[10]); b = mx3(b, s[11], s[12]); a = mx3(a, s[13], s[14]);
  return mx3(a, b, s[15]);
}
DI v4u pk8(const f16v& v, int o) { return (v4u){pk(v[o], v[o + 1]), pk(v[o + 2], v[o + 3]), pk(v[o + 4], v[o + 5]), pk(v[o + 6], v[o + 7])}; }
#define SPIN_LOG 12
constexpr u32 SPIN_CAP = 1u << SPIN_LOG;
DI u32 fresh(u32 v) { asm volatile("" : "+v"(v)); return v; }
DI u32 sfresh(u32 v) { asm volatile("" : "+s"(v)); return v; }
constexpr u32 EMPTY = 0xffffffffu;
DI u32 umax3(u32 a, u32 b, u32 c) { return max(max(a, b), c); }
DI u32 canon(u32 v) { return min(v, EMPTY - 1); }
DI v4u canon(v4u v) { return (v4u){canon(v.x), canon(v.y), canon(v.z), canon(v.w)}; }
DI bool empty2(const v4u& a, const v4u& b) { return __any(max(umax3(a.x, a.y, a.z), umax3(umax3(a.w, b.x, b.y), b.z, b.w)) == EMPTY); }

#define RETRY_ARG
#define RETRY_PASS
#define RETRY_INC()
template <int P, int N, int NX> DI u32 validate1(v4u (&x)[NX]) {
  v4u &a = x[P % NX], &b = x[(P + 1) % NX];
  wait_vm<N>(a, b);
  u32 v = umax3(umax3(a.x, a.y, a.z), a.w, b.x); pin(v); return v;
}
template <int P, int NX> DI u64 validate2(u32 v, v4u (&x)[NX]) { const v4u& b = x[(P + 1) % NX]; u64 e = __ballot(max(umax3(v, b.y, b.z), b.w) == EMPTY); asm volatile("" : "+s"(e)); return e; }
template <int P, int NX, class FI> DI void validate3(u64 e, v4u (&x)[NX], FI&& issue RETRY_ARG) {
  v4u &a = x[P % NX], &b = x[(P + 1) % NX];
  if (__builtin_expect(e != 0, 0)) {
#pragma unroll 1
    for (u32 spins = 0; spins < SPIN_CAP; ++spins) {
      __builtin_amdgcn_s_sleep(4); RETRY_INC();
      issue(ic<P>{}, a); issue(ic<P + 1>{}, b);
      wait_vm<0>(a, b);
      if (!empty2(a, b)) break;
    }
  }
}
template <int N> DI void wait_lgkm() { __builtin_amdgcn_s_waitcnt(0xc07f | (N << 8)); }
constexpr int cmin(int a, int b) { return a < b ? a : b; }
constexpr int issued(int s, int KS, int LOOK, int INIT) { return s == 0 ? INIT : cmin(cmin(INIT + 2 * s, ((s - 1) & ~1) + LOOK), KS); }
constexpr int DEP = 5;
template <int NT, int KS, int KM> constexpr int ord_s(int i) { return i < KM * NT ? i / NT : KM + (i - KM * NT) % (KS > KM ? KS - KM : 1); }
template <int NT, int KS, int KM> constexpr int ord_t(int i) { return i < KM * NT ? i % NT : (i - KM * NT) / (KS > KM ? KS - KM : 1); }
template <int NT, int KS, int LOOK, int NX, bool SWAPLAST, bool VAL, int INIT = LOOK, int KM = KS, class FI, class FA, class FH>
DI void gemm(f16v* acc, v4u (&x)[NX], FI&& issue, FA&& addr, FH&& hook RETRY_ARG) {
  constexpr int N1 = KS * NT;
  v4u ring[DEP + 1];
  u32 vv; u64 ve;
  auto unit = [&](auto i) { return addr(ic<ord_s<NT, KS, KM>(i) * NT + ord_t<NT, KS, KM>(i)>{}); };
  sfor<DEP>([&](auto i) { ring[i] = trfrag(unit(i)); });
  SBAR();
  if constexpr (LOOK > 0 && VAL) { vv = validate1<0, INIT - 2>(x); ve = validate2<0>(vv, x); validate3<0>(ve, x, issue RETRY_PASS); }
  if constexpr (LOOK > 0 && !VAL) wait_vm<INIT - 2>(x[0], x[1]);
  sfor<N1>([&](auto i_) {
    constexpr int i = i_, s = ord_s<NT, KS, KM>(i), t = ord_t<NT, KS, KM>(i);
    if constexpr ((i & 1) == 0 && i + DEP <= N1) { wait_lgkm<2 * DEP - 4>(); SBAR(); }
    if constexpr (SWAPLAST && t == NT - 1) acc[t] = mfma(x[s % NX], ring[i % (DEP + 1)], acc[t]);
    else acc[t] = mfma(ring[i % (DEP + 1)], x[s % NX], acc[t]);
    SBAR();
    hook(i_);
    SBAR();
    if constexpr (i + DEP < N1) ring[(i + DEP) % (DEP + 1)] = trfrag(unit(ic<i + DEP>{}));
    if constexpr (LOOK > 0 && i < KM * NT) {
      constexpr int f0 = issued(s, KS, LOOK, INIT), f1 = issued(s + 1, KS, LOOK, INIT);
      static_assert(NT > 2 || (s & 1) == 0 || f1 == f0, "two-tile loops issue in even steps only");
      if constexpr (t < 2 && f0 + t < f1) { SBAR(); issue(ic<f0 + t>{}, x[(f0 + t) % NX]); }
      constexpr int G = KM == KS ? 2 * NT : NT, g = i % G, P = (i / G + 1) * 2;
      static_assert(KM == KS || 2 * KM == KS, "tail = half of the steps");
      if constexpr (P < KS) {
        if constexpr (VAL) {
          if constexpr (g == G - 3) { SBAR(); vv = validate1<P, f1 - (P + 2)>(x); }
          if constexpr (g == G - 2) { SBAR(); ve = validate2<P>(vv, x); }
          if constexpr (g == G - 1) { SBAR(); validate3<P>(ve, x, issue RETRY_PASS); }
        } else if constexpr (g == G - 1) { SBAR(); wait_vm<f1 - (P + 2)>(x[P % NX], x[(P + 1) % NX]); }
      }
    }
    SBAR();
  });
}

#define STAMP() do {} while (0)
#define STAMPCLK(i) do {} while (0)
DI f2 ln_stats(float s1, float s2) {
  const float mean = s1 * (1.0f / 512.0f), var = __builtin_fmaxf(s2 * (1.0f / 512.0f) - mean * mean, 0.0f);
  return (f2){-mean, __builtin_amdgcn_sqrtf(var + 1e-5f)};
}
DI u32 trbase(u32 lane) { return (4 * (lane >> 5) + ((lane & 15) >> 2)) * 64 + ((lane >> 4) & 1) * 32 + (lane & 3) * 8; }
DI float ex2(float x) { return __builtin_amdgcn_exp2f(x); }
DI v4u expk8(const f16v& s, int o) { return (v4u){pk(ex2(s[o]), ex2(s[o + 1])), pk(ex2(s[o + 2]), ex2(s[o + 3])), pk(ex2(s[o + 4]), ex2(s[o + 5])), pk(ex2(s[o + 6]), ex2(s[o + 7]))}; }
DI v4u aux_tok(f2 st, u32 h) {
  const float v = h ? st.y : st.x; const u32 a = pk(v, v) & 0xffffu; const float r = v - blo(a); const u32 b = pk(r, r) & 0xffffu; const float r2 = r - blo(b);
  return (v4u){a | (b << 16), h ? pk(r2, 0.0f) : a, 0u, 0u};
}
DI v4u aux_col(u32 v) { return (v4u){(v & 0xffffu) | (v << 16), v >> 16, 0u, 0u}; }

struct Attn {
  v4u q[2], kf, v0, v1, p0, p1, t0, t1; f16v o[2], nm[2], sc[2]; u32 ka, va, vb, vstr, l16;
  DI void init(u32 lane, int qb) {
    l16 = lane * 16;
    const bool real = (lane & 16) == 0;
    ka = L_KV + qb * 4096 + l16; vstr = real ? 4096u : 512u;
    va = real ? L_KV + qb * 4096 + 2048 + l16 : L_ONES + qb * 512 + (lane & 15) * 16; vb = real ? va + 1024 : va;
  }
  DI f16v addmask() { f16v c; sfor<4>([&](auto g) { const f4 t = ldsr<f4>(L_TA + g * 1024 + l16); sfor<4>([&](auto i) { c[4 * g + i] = t[(int)i]; }); }); return c; }
  template <int D, int MASK, bool QK, bool KN, int PV> DI void stage(u32 a0, u32 a1, u32 kn) {
    const f16v& si = sc[D];
    float e[16];
    sfor<4>([&](auto i) { e[i] = ex2(si[(int)i]); });
    SBAR();
    wait_lgkm<0>();
    if constexpr (QK) sc[1 - D] = mfma(kf, q[1 - D], nm[1 - D]);
    SBAR();
    if constexpr (KN) kf = ldsr<v4u>(kn);
    sfor<4>([&](auto i) { e[4 + i] = ex2(si[4 + i]); });
    SBAR();
    if constexpr (PV == 1) o[1 - D] = mfma(v0, p0, o[1 - D]);
    if constexpr (PV == 2) o[1 - D] = mfma(v0, p0, (f16v){});
    SBAR();
    v0 = ldsr<v4u>(a0);
    sfor<4>([&](auto i) { e[8 + i] = ex2(si[8 + i]); });
    SBAR();
    if constexpr (PV) o[1 - D] = mfma(v1, p1, o[1 - D]);
    SBAR();
    v1 = ldsr<v4u>(a1);
    sfor<4>([&](auto i) { e[12 + i] = ex2(si[12 + i]); });
    SBAR();
    sfor<4>([&](auto i) { p0[(int)i] = pk(e[2 * i], e[2 * i + 1]); });
    sfor<4>([&](auto i) { p1[(int)i] = pk(e[8 + 2 * i], e[9 + 2 * i]); });
    if constexpr (MASK == 1) { pin(t0); pin(t1); p0 &= ~t0; p1 &= ~t1; }
    if constexpr (MASK == 2) { p0 &= t0; p1 &= t1; }
    pin(p0); pin(p1);
    SBAR();
  }
  template <class F> DI void diag(F&& mid) {
    const f16v c = addmask();
    v4u kd[2];
    sfor<2>([&](auto d) { kd[d] = ldsr<v4u>(ka + 8 * 4096 + d * 1024); });
    t0 = ldsr<v4u>(L_TB + l16); t1 = ldsr<v4u>(L_TB + 1024 + l16);
    SBAR();
    sfor<2>([&](auto d) { sc[d] = mfma(kd[d], q[d], c); });
    va = fresh(va + 8 * vstr); vb = fresh(vb + 8 * vstr);
    SBAR();
    sfor<2>([&](auto d) {
      const float mx = hmax(max16(sc[d], sc[d][0]));
      sfor<16>([&](auto r) { nm[d][(int)r] = -mx; });
      pin(nm[d]);
      sc[d] = mfma(kd[d], q[d], nm[d]);
      SBAR();
    });
    stage<0, 1, false, false, 0>(va, vb, 0);
    mid();
    stage<1, 1, false, false, 2>(va + 256, vb + 256, 0);
  }
  template <class F> DI void rest(F&& hint) {
    sc[0] = mfma(ldsr<v4u>(ka + 7 * 4096), q[0], nm[0]); kf = ldsr<v4u>(ka + 7 * 4096 + 1024);
    sfor<8>([&](auto i) {
      constexpr int j = 7 - i;
      va = fresh(va - vstr); vb = fresh(vb - vstr);
      if constexpr (j == E3) hint();
      stage<0, j == 0 ? 2 : 0, true, (j > 0), j == 7 ? 2 : 1>(va, vb, ka + (j - 1) * 4096);
      stage<1, j == 0 ? 2 : 0, (j > 0), (j > 0), 1>(va + 256, vb + 256, ka + (j - 1) * 4096 + 1024);
    });
    o[1] = mfma(v0, p0, o[1]); o[1] = mfma(v1, p1, o[1]);
  }
  template <int D> DI v4u finish(int jmin) {
    f16v& oo = o[D];
    float big = __builtin_fabsf(oo[0]); sfor<7>([&](auto r) { big += __builtin_fabsf(oo[r + 1]); });
    if (__builtin_expect(!__all(oo[8] <= 0x1p60f && big <= 0x1p126f), 0)) {
      auto scores = [&](int j) {
        f16v c = addmask();
        if (j != 8) sfor<16>([&](auto r) { c[(int)r] = (j == 0 && c[(int)r] == 0.0f) ? -__builtin_inff() : 0.0f; });
        return mfma(ldsr<v4u>(ka + j * 4096 + D * 1024), q[D], c);
      };
      float mx = -__builtin_inff();
#pragma unroll 1
      for (int j = jmin; j <= 8; ++j) mx = max16(scores(j), mx);
      mx = hmax(mx);
      oo = (f16v){};
#pragma unroll 1
      for (int j = jmin; j <= 8; ++j) {
        const f16v s = scores(j) - mx;
        oo = mfma(ldsr<v4u>(va + j * vstr + D * 256), expk8(s, 0), oo); oo = mfma(ldsr<v4u>(vb + j * vstr + D * 256), expk8(s, 8), oo);
      }
    }
    const float rl = __builtin_amdgcn_rcpf(oo[8]);
    sfor<8>([&](auto r) { oo[(int)r] *= rl; });
    return canon(pk8(oo, 0));
  }
};

constexpr int w2_kstep(int i) { return ((i % 14) / 2) * 8 + 2 * (i / 14) + (i & 1); }
constexpr int w2r_slot(int i) { return i < 6 ? 6 + 2 * i : 38 + 2 * (i - 6); }
constexpr int w2_pos(int i) { return i < 38 ? (i + 32) % 38 : (i - 6) % 39; }

__global__ void __launch_bounds__(512, 2) fwd(Params p) {
  extern __shared__ __attribute__((aligned(16))) unsigned char smem[];
  const int w0 = __builtin_amdgcn_readfirstlane(threadIdx.x >> 6);
#define IDS() const int w = sfresh(w0), bid = sfresh(blockIdx.x), grp = (bid & 7) * 2 + (bid >> 7), m = (bid >> 3) & 15, cu = grp * 16 + m, hw = w & 3, cb = m & 7, th = m >> 3, \
              rt = w & 1, kh = (w >> 1) & 1, T13 = 4 * grp + hw, T24 = 4 * grp + 2 * th + rt, rot = (4 * (m & 1) + 2 * (grp & 1) + (E1 ? m >> 1 : (m >> 1) & 1)) & 7, rot2 = (th + 2 * grp + cb) & 3, rot4 = (cb + 2 * th + 4 * (grp & 1)) % 7; (void)rot; (void)rot2; (void)rot4; (void)cu; (void)cb; (void)kh; (void)T13; (void)T24
  const v4i rws = mk_rsrc(p.ws, WS_END);
  const u32 lane0 = threadIdx.x & 63;
  STAMP(); STAMPCLK(0);
  v2u tagv;
  auto load_tags = [&]() { IDS(); const u32 g = grp + (lane0 < 3 ? lane0 : 0); bld8<0, true>(tagv, (g < 16 ? g : grp) * 256, rws, WS_EPOCH); };
  auto fix_tags = [&]() { pin(tagv); tagv = __builtin_bit_cast(v2u, __builtin_bit_cast(u64, tagv) + 1); };
  auto tag0 = [&]() { return (v2u){(u32)__builtin_amdgcn_readfirstlane(tagv.x), (u32)__builtin_amdgcn_readfirstlane(tagv.y)}; };
  auto publish = [&](int sid) { IDS(); const u32 lane = fresh(lane0); if (lane < 16) bst8(tag0(), lane * 128 + m * 8, rws, WS_FLAG + (sid * 256 + grp * 16) * 128); };
  auto await = [&](int sid, u32 first, u32 mask) {
    IDS(); const u64 t0 = __builtin_bit_cast(u64, tag0()); const u32 vo = (fresh(lane0) & mask) * 8;
#pragma unroll 1
    for (u32 spins = 0; spins < SPIN_CAP; ++spins) {
      v2u f; bld8<0, true>(f, vo, rws, WS_FLAG + (sid * 256 + cu) * 128 + first * 8); wait_vm<0>(f);
      if (__all(__builtin_bit_cast(u64, f) == t0)) break;
      __builtin_amdgcn_s_sleep(2);
    }
  };
  const u32 wrow = lane0 >> 2, wch = (lane0 & 3) * 16;
  if (w0 >= 4) {
    const v4i ro = mk_rsrc(p.wo, 512 * KB), r1 = mk_rsrc(p.w1, 1792 * KB), r2 = mk_rsrc(p.w2, 1792 * KB);
    u32 vo24, vo1;
    { IDS(); vo24 = wrow * 1024 + wch + cb * 128; vo1 = wrow * 3584 + wch + m * 256; }
    auto stage_w1 = [&](int s0, int s1) {
      IDS();
      if (m >= 14) return;
#pragma unroll 1
      for (int s = s0 + hw; s < s1; s += 4) {
        const u32 so = ((s + 17 + 4 * rot) & 31) * 16 * 3584;
        dma16(L_W1 + s * 4096, vo1, r1, so); dma16(L_W1 + s * 4096 + 1024, vo1, r1, so + 64); dma16(L_W1 + s * 4096 + 2048, vo1, r1, so + 128); dma16(L_W1 + s * 4096 + 3072, vo1, r1, so + 192);
      }
    };
    auto w2_row = [&](int i, int rot4) { const int t = (i * 37) >> 9, r = i - 14 * t, j = (r >> 1) + rot4; return (u32)(((j >= 7 ? j - 7 : j) * 8 + 2 * t + (r & 1)) * 16384); };
    auto stage_w2 = [&](int i0, int i1, int p0) {
      IDS();
#pragma unroll 1
      for (int i = i0 + hw; i < i1; i += 4) {
        const u32 la = (i + p0) * 4096, so = w2_row(i, rot4);
        dma16(la, vo24, r2, so); dma16(la + 1024, vo24, r2, so + 64); dma16(la + 2048, vo24, r2, so + 56 * 16384); dma16(la + 3072, vo24, r2, so + 56 * 16384 + 64);
      }
    };
    const v4u ones = {0x3f803f80u, 0x3f803f80u, 0x3f803f80u, 0x3f803f80u}, empt = {EMPTY, EMPTY, EMPTY, EMPTY};
    auto tables = [&]() {
      IDS(); const u32 lane = fresh(lane0), l16 = lane * 16; const int dq = (int)(lane & 31) - 4 * (int)(lane >> 5);
      sfor<4>([&](auto g) { f4 t; sfor<4>([&](auto i) { t[(int)i] = 8 * g + i <= dq ? 0.0f : -__builtin_inff(); }); ldsw<f4>(L_TA + g * 1024 + l16, t); });
      sfor<2>([&](auto g) { v4u b; sfor<4>([&](auto d) { const int c = 16 * g + 8 * (d >> 1) + 2 * (d & 1); b[(int)d] = (c > dq ? 0xffffu : 0u) | (c + 1 > dq ? 0xffff0000u : 0u); });
                            ldsw<v4u>(L_TB + g * 1024 + l16, b); });
      sfor<6>([&](auto g) { ldsw<v4u>(L_ONES + g * 1024 + l16, grp + g / 2 >= 2 ? ones : (v4u){}); });
    };
    auto cs_write = [&](const f16v& a, u32 dst) {
      const u32 lane = fresh(lane0);
      if ((lane & 31) == 0) sfor<4>([&](auto g) { v4u q; sfor<4>([&](auto i) { const float v = a[4 * g + i]; const u32 hi = pk(v, v) & 0xffffu; q[(int)i] = hi | (pk(0.0f, v - blo(hi)) & 0xffff0000u); });
                                                  ldsw<v4u>(dst + (8 * g + 4 * (lane >> 5)) * 4, q); });
    };
    auto put_stats = [&](f2 a, f2 b, u32 tok2) { ldsw<f4>(L_SD + tok2 * 16, (f4){a.x, a.y, b.x, b.y}); ldsw<f2>(L_RS + tok2 * 8, (f2){__builtin_amdgcn_rcpf(a.y), __builtin_amdgcn_rcpf(b.y)}); };
    v4u keep[24], w2r[30];
    {
      IDS();
      const u32 l16 = lane0 * 16;
      if (hw < 3) {
        const v4i rq = mk_rsrc(p.wqkv, 1536 * KB);
        const u32 vo = (8 * ((wrow >> 2) & 1) + 4 * (wrow >> 3) + (wrow & 3)) * 3072 + wch + m * 64 + hw * 1024;
        auto chunk = [&](int c) { const u32 so = 64 * ((c + rot) & 7) * 3072, la = L_WQ + c * 12288 + hw * 1024; sfor<4>([&](auto i) { dma16(la + i * 3072, vo, rq, so + i * 16 * 3072); }); };
        chunk(0); chunk(1);
        BARRIER();
        f16v a = {}; const u32 b0 = L_WQ + hw * 1024 + trbase(lane0);
        auto landed = [&](int c) {
          ldsw<u32>(L_FLG + hw * 4, c + 1);
          v4u q[4]; sfor<4>([&](auto i) { q[i] = trfrag(b0 + (4 * c + i) * 3072); }); SBAR();
          sfor<4>([&](auto i) { a = mfma(q[i], ones, a); });
        };
        WAIT_VM(4); ldsw<u32>(L_FLG + hw * 4, 1);
        chunk(2);
        landed(0);
#pragma unroll 1
        for (int c = 1; c < 6; ++c) { chunk(c + 2); WAIT_VM(8); landed(c); }
        WAIT_VM(4); landed(6);
        load_tags();
        WAIT_VM(1); landed(7);
        cs_write(a, L_CSQ + hw * 128);
        WAIT_VM(0); fix_tags();
      } else {
        v4u xr[8];
        const v4i rx = mk_rsrc(p.x, 2 * MB); const u32 vx = (lane0 >> 3) * 1024 + (lane0 & 7) * 16, so = (grp * 128 + m * 8) * 1024;
        sfor<8>([&](auto i) { bld16<i * 128, false>(xr[i], vx, rx, so); });
        if (lane0 < 4) ldsw<u32>(L_FLG + lane0 * 4, lane0 == 3 ? 255u : 0u);
        BARRIER();
        load_tags();
        u32 dd[6] = {};
        {
          touch(dd[3], lane0 < 56 ? lane0 * 128 : 0x7fffffffu, r1, bid * 7168); touch(dd[4], lane0 < 56 ? lane0 * 128 : 0x7fffffffu, r2, bid * 7168); touch(dd[5], lane0 < 16 ? lane0 * 128 : 0x7fffffffu, ro, bid * 2048);
        }
        tables();
        WAIT_VM(0); fix_tags();
        sfor<6>([&](auto i) { pin(dd[i]); });
        sfor<8>([&](auto i) { pin(xr[i]); });
        float s1 = 0.f, s2 = 0.f;
        sfor<32>([&](auto i) { const u32 eu = xr[i / 4][i % 4]; const bf2 e = __builtin_bit_cast(bf2, eu);
                               s1 = __builtin_amdgcn_fdot2_f32_bf16(e, __builtin_bit_cast(bf2, 0x3f803f80u), s1, false); s2 = __builtin_amdgcn_fdot2_f32_bf16(e, e, s2, false); });
        sfor<3>([&](auto i) { s1 += __shfl_xor(s1, 1 << i); s2 += __shfl_xor(s2, 1 << i); });
        if ((lane0 & 7) == 0) bst8(__builtin_bit_cast(v2u, ln_stats(s1, s2)), lane0, rws, WS_ST0 + grp * 1024 + m * 64);
        sfor<4>([&](auto i) { const u32 sf = WS_OF + ((4 * grp + i) * 32 + 2 * m) * 1024; bst16<0>(empt, l16, rws, sf); bst16<1024>(empt, l16, rws, sf); });
        WAIT_VM(0);
        publish(2);
        __builtin_amdgcn_s_sleep(32);
        await(2, 0, 15);
        v4u q; bld16<0, true>(q, l16, rws, WS_ST0 + grp * 1024); wait_vm<0>(q);
        const f4 s = __builtin_bit_cast(f4, q); put_stats((f2){s[0], s[1]}, (f2){s[2], s[3]}, lane0);
      }
    }
    sfor<2>([&](auto L_) {
      constexpr int L = L_;
      IDS();
      const u32 lane = fresh(lane0), l16 = lane * 16;
      auto gather_stats = [&](int buf) {
        __builtin_amdgcn_s_setprio(3);
        v4u q[4]; const u32 so = WS_ST + buf * 256 * KB + T13 * 4096;
#pragma unroll 1
        for (u32 spins = 0; spins < SPIN_CAP; ++spins) {
          bld16<0, true>(q[0], l16, rws, so); bld16<1024, true>(q[1], l16, rws, so); bld16<2048, true>(q[2], l16, rws, so); bld16<3072, true>(q[3], l16, rws, so);
          wait_vm<0>(q[0], q[1]); wait_vm<0>(q[2], q[3]);
          if (!empty2(q[0], q[1]) && !empty2(q[2], q[3])) break;
          __builtin_amdgcn_s_sleep(4);
        }
        float s[4];
        sfor<4>([&](auto i) { const u32 a = q[0][(int)i], b = q[1][(int)i], c = q[2][(int)i], d = q[3][(int)i];
                              float v = fadd(fadd(bf(a), bf(b)), fadd(bf(c), bf(d)));
                              auto r = __builtin_amdgcn_permlane16_swap(fb(v), fb(v), false, false); const u32 r0 = r[0], r1 = r[1];
                              const f2 hh = halves(fadd(bf(r0), bf(r1))); s[i] = fadd(hh.x, hh.y); });
        if (lane < 16) put_stats(ln_stats(s[0], s[1]), ln_stats(s[2], s[3]), hw * 16 + lane);
        __builtin_amdgcn_s_setprio(0);
      };
      auto fill_x = [&](int buf) { const u32 so = WS_XF + buf * 2 * MB + (T24 * 32 + cb * 4 + kh * 2) * 1024; bst16<0>(empt, l16, rws, so); bst16<1024>(empt, l16, rws, so);
                                   if (lane < 16) bst16<0>(empt, l16, rws, WS_ST + buf * 256 * KB + (T24 * 16 + cb * 2 + kh) * 256); };
      auto stage_wo = [&](int s0, int s1) {
#pragma unroll 1
        for (int s = s0 + hw; s < s1; s += 4) { const u32 so = ((s & 16) + ((s + 4 * rot2) & 15)) * 16384; dma16(L_WO + s * 2048, vo24, ro, so); dma16(L_WO + s * 2048 + 1024, vo24, ro, so + 64); } };
      auto w2_get = [&](v4u* r, int s0) { const u32 so = w2_row(s0, rot4) + (hw & 1) * 16384 + (hw >> 1) * 56 * 16384; bld16<0, false>(r[0], vo24, r2, so); bld16<64, false>(r[1], vo24, r2, so); };
      auto w2_set = [&](v4u* r, int s0, u32 ta) { pin(r[0]); pin(r[1]); const u32 o = ta + ((hw & 1) ? w2_pos(s0 + 1) : w2_pos(s0)) * 4096; ldsw<v4u>(o, r[0]); ldsw<v4u>(o + 1024, r[1]); };
      auto w2_regs = [&](auto i0, auto n) { sfor<n>([&](auto j) { constexpr int i = i0 + j; w2_get(&w2r[2 * i], w2r_slot(i)); }); };
      auto w2_put = [&](auto i0, auto n) { const u32 ta = fresh(l16 + (hw >> 1) * 2048); sfor<n>([&](auto j) { constexpr int i = i0 + j; w2_set(&w2r[2 * i], w2r_slot(i), ta); }); };
      v4u st1[24];
      auto w1_pos = [&](int i) { const int j = hw + 4 * i; return j < 11 ? j : j == 11 ? 23 : j < 16 ? j - 1 : j + 8; };
      auto w1_get = [&](auto i0, auto n) { if (m < 14) sfor<n>([&](auto j) { constexpr int i = i0 + j; const u32 so = ((w1_pos(i) + 17 + 4 * rot) & 31) * 16 * 3584;
                                                                             sfor<4>([&](auto t) { bld16<t * 64, false>(st1[4 * i + t], vo1, r1, so); }); }); };
      auto w1_put = [&](auto i0, auto n) { if (m < 14) sfor<n>([&](auto j) { constexpr int i = i0 + j; const u32 o = fresh(l16) + L_W1 + w1_pos(i) * 4096;
                                                                             sfor<4>([&](auto t) { pin(st1[4 * i + t]); ldsw<v4u>(o + t * 1024, st1[4 * i + t]); }); }); };
      f16v cs = {};
      auto w1_colsum = [&](int p0, int p1) {
        const u32 b0 = L_W1 + hw * 1024 + trbase(lane);
#pragma unroll 1
        for (int z = p0; z < p1; z += 4) { v4u q[4]; sfor<4>([&](auto i) { q[i] = trfrag(b0 + (z + i < p1 ? z + i : p0) * 4096); }); SBAR();
                                           sfor<4>([&](auto i) { cs = mfma(q[i], z + i < p1 ? ones : (v4u){}, cs); }); }
      };
      auto halo = [&]() {
        __builtin_amdgcn_s_setprio(3);
        if (4 * grp + 2 * hw >= 8) {
          await(L, 2 * hw, 1);
          sfor<8>([&](auto i) { dma16<true>(L_KV + hw * 8192 + i * 1024, l16, rws, WS_KV + L * 4 * MB + ((4 * grp - 8 + 2 * hw + (i >> 2)) * 16 + m) * 4096 + (i & 3) * 1024); });
        } else sfor<8>([&](auto i) { ldsw<v4u>(L_KV + hw * 8192 + i * 1024 + l16, (v4u){}); });
        __builtin_amdgcn_s_setprio(0);
      };
      auto hint = [&](int sid) { if (hw == 3) { __builtin_amdgcn_s_setprio(3); publish(sid); __builtin_amdgcn_s_setprio(0); } };
      v2u hf;
      auto ask = [&](int sid) { if (hw == 2) bld8<0, true>(hf, (lane & 15) * 8, rws, WS_FLAG + (sid * 256 + cu) * 128); };
      auto hinted = [&](int sid) { if (hw == 2) { __builtin_amdgcn_s_setprio(3); wait_vm<0>(hf); if (!__all(__builtin_bit_cast(u64, hf) == __builtin_bit_cast(u64, tag0()))) await(sid, 0, 15); __builtin_amdgcn_s_setprio(0); } };
      if constexpr (L == 0) BARRIER(); else HBAR();
      if (L == 0) sfor<24>([&](auto i) { keep[i] = ldsr<v4u>(L_WQ + hw * 24 * KB + i * 1024 + l16); });
      else if (hw == 3) tables();
      w1_get(ic<3>{}, ic<3>{});
      HBAR();
      __builtin_amdgcn_s_sleep(8);
      halo();
      fill_x(2 * L);
      WAIT_VM(3); BARRIER();
      stage_wo(0, 32);
      stage_w1(15, 23);
      if (E3 == 0) w1_get(ic<0>{}, ic<3>{});
      WAIT_VM(0);
      HBAR();
      hint(3 + L);
      if (E3) { w1_get(ic<0>{}, ic<3>{}); WAIT_VM(0); }
      BARRIER();
      ask(3 + L);
      w1_put(ic<0>{}, ic<2>{});
      if (hw < 3) w1_put(ic<2>{}, ic<1>{});
      hinted(3 + L);
      BARRIER();
      if (L == 0 && m < 14) w1_colsum(0, 11);
      HBAR();
      hint(5 + L);
      if (L == 0 && m < 14) w1_colsum(15, 23);
      BARRIER();
      ask(5 + L);
      w1_put(ic<4>{}, ic<2>{});
      if (hw == 3) w1_put(ic<2>{}, ic<1>{});
      hinted(5 + L);
      BARRIER();
      if (L == 0 && hw == 0 && m < 14) { const u32 v = ((const GAS u32*)p.b1)[m * 64 + lane]; ldsw<v2u>(L_B1 + lane * 8, (v2u){(v & 0xffffu) | (v << 16), (v >> 16) | (v & 0xffff0000u)}); }
      gather_stats(2 * L);
      w1_put(ic<3>{}, ic<1>{});
      if (L == 0 && m < 14) w1_colsum(23, 32);
      HBAR();
      if (L == 0 && m < 14) { w1_colsum(11, 15); cs_write(cs, L_CS1 + hw * 128); }
      HBAR();
      stage_w2(0, 6, 32);
      v4u st2[20];
      if (L == 0) { w2_regs(ic<0>{}, ic<6>{}); fill_x(1); const u32 so = WS_OF + 2 * MB + (T13 * 32 + 2 * m) * 1024; bst16<0>(empt, l16, rws, so); bst16<1024>(empt, l16, rws, so); WAIT_VM(0); }
      else { sfor<10>([&](auto i) { w2_get(&st2[2 * i], 18 + 2 * i); }); WAIT_VM(20); }
      BARRIER();
      const u32 ta = fresh(l16 + (hw >> 1) * 2048);
      hint(8 + 4 * L);
      w2_put(ic<0>{}, ic<6>{});
      HBAR();
      hint(9 + 4 * L);
      ask(8 + 4 * L);
      if (L == 0) { if (hw != 2) stage_w2(18, 22, -6); } else { if (hw >= 2) WAIT_VM(1); else WAIT_VM(0); sfor<3>([&](auto i) { w2_set(&st2[2 * i], 18 + 2 * i, ta); }); }
      hinted(8 + 4 * L);
      BARRIER();
      hint(10 + 4 * L);
      if (L == 1) sfor<3>([&](auto i) { w2_set(&st2[6 + 2 * i], 24 + 2 * i, ta); }); else if (hw == 2) __builtin_amdgcn_s_sleep(8);
      ask(9 + 4 * L);
      HBAR();
      hint(11 + 4 * L);
      hinted(9 + 4 * L);
      HBAR();
      if (L == 0) { if (hw == 2) stage_w2(18, 22, -6); stage_w2(22, 38, -6); } else sfor<4>([&](auto i) { w2_set(&st2[12 + 2 * i], 30 + 2 * i, ta); });
      ask(10 + 4 * L);
      if (L == 0) WAIT_VM(0);
      HBAR();
      hinted(10 + 4 * L);
      HBAR();
      ask(11 + 4 * L);
      if (L == 0) { w2_regs(ic<6>{}, ic<9>{}); WAIT_VM(0); }
      w2_put(ic<6>{}, ic<7>{});
      hinted(11 + 4 * L);
      HBAR();
      w2_put(ic<13>{}, ic<2>{});
      HBAR();
      HBAR();
      const u32 ka = fresh(L_WQ + hw * 24 * KB + (8 * ((lane >> 4) & 1) + 4 * (lane >> 5) + ((lane >> 2) & 3)) * 64 + (lane & 3) * 16);
      if (L == 0 && hw < 2) sfor<12>([&](auto i) { ldsw<v4u>(ka + i * 1024, keep[i]); });
      HBAR();
      if (L == 0) hint(7);
      if (L == 0 && hw < 2) sfor<12>([&](auto i) { ldsw<v4u>(ka + (12 + i) * 1024, keep[12 + i]); });
      BARRIER();
      if constexpr (L == 0) {
        ask(7);
        if (hw >= 2) sfor<24>([&](auto i) { ldsw<v4u>(ka + i * 1024, keep[i]); });
        hinted(7);
        BARRIER();
        gather_stats(1);
      }
    });
    STAMP();
    return;
  }
  v4u res[2], b2p[2];
  v4u qf[2];
  struct Aux { v4u tf; u32 c[4]; float rs; f2 st; f4 rv[4]; };
  auto aux_read = [&](Aux& x, bool ffn) {
    IDS();
    const u32 lane = fresh(lane0), h = lane >> 5, t32 = lane & 31;
    x.st = ldsr<f2>(L_SD + hw * 256 + t32 * 8);
    x.rs = ldsr<float>(L_RS + hw * 128 + t32 * 4);
    if (ffn) { const u32 lc = fresh(L_CS1 + h * 512 + t32 * 4); sfor<4>([&](auto t) { x.c[t] = ldsr<u32>(lc + t * 128); }); }
    else { const u32 lc = fresh(L_CSQ + t32 * 4), lr = fresh(L_RS + hw * 128 + 16 * h);
           sfor<3>([&](auto t) { x.c[t] = ldsr<u32>(lc + t * 128); });
           sfor<4>([&](auto g) { x.rv[g] = ldsr<f4>(lr + 32 * g); }); }
  };
  auto aux_make = [&](Aux& x, bool ffn) { const u32 h = fresh(lane0) >> 5; x.tf = aux_tok(x.st, h); if (!ffn && h) x.tf = (v4u){}; pin(x.tf); };
  v4u kv[4]; u32 lk;
  auto kv_epilogue = [&](u32 so, const Aux& x, f16v& ak, f16v& av) {
    const u32 l16 = fresh(lane0) * 16;
    ak *= x.rs;
    kv[0] = pk8(ak, 0); kv[1] = pk8(ak, 8); bst16<0>(kv[0], l16, rws, so); bst16<1024>(kv[1], l16, rws, so);
    SBAR();
    sfor<16>([&](auto r) { av[(int)r] *= x.rv[r / 4][r % 4]; });
    kv[2] = pk8(av, 0); kv[3] = pk8(av, 8); bst16<2048>(kv[2], l16, rws, so); bst16<3072>(kv[3], l16, rws, so);
    lk = fresh(L_KV + (8 + w0) * 4096 + l16);
  };
  auto q_epilogue = [&](const Aux& x, f16v aq) { aq *= x.rs * (0.25f * 1.4426950408889634f); qf[0] = pk8(aq, 0); qf[1] = pk8(aq, 8); };
  u32 kvf; { IDS(); kvf = (lane0 == 1 || lane0 == 2) && grp + lane0 < 16 ? WS_FLAG + cu * 128 + lane0 * (16 * 128 - 32) + 64 + hw * 8 : 0x7fffffffu; }
  auto kv_flag = [&](int L) { WAIT_VM(0); bst8(tagv, kvf, rws, L * 256 * 128); };
  {
    IDS();
    f16v acc1[3] = {};
    const u32 lane = lane0, h = lane >> 5, t32 = lane & 31;
    const v4i rx = mk_rsrc(p.x, 2 * MB);
    const u32 vo = wrow * 1024 + (((lane & 3) ^ ((lane >> 4) & 3)) * 16), so = T13 * 32 * 1024, xs = hw * 12 * KB;
    auto issue = [&](int u) { const u32 o = u < 16 ? so + ((u + 2 * rot) & 15) * 64 : T24 * 32 * 1024 + cb * 128 + kh * 64;
                              dma16(xs + (u % 6) * 2048, vo, rx, o); dma16(xs + (u % 6) * 2048 + 1024, vo, rx, o + 16384); };
    issue(0); issue(1);
    BARRIER();
    load_tags();
    v2u bb[4];
    { const v4i rb = mk_rsrc(p.b2, 1024); const u32 bo = (cb * 64 + kh * 32 + 4 * h) * 2; sfor<4>([&](auto i) { bld8<i * 16, false>(bb[i], bo, rb, 0); }); }
    issue(2); issue(3);
    const u32 xa0 = xs + t32 * 64 + ((h ^ ((t32 >> 2) & 3)) * 16), xa1 = xa0 ^ 32;
    const u32 wb0 = fresh(L_WQ + trbase(lane)), wb1 = fresh(wb0 + 48 * KB);
    auto addr = [&](auto i) { constexpr int o = i * 1024; return o < 48 * KB ? wb0 + o : wb1 + (o - 48 * KB); };
    auto chunk_wait = [&](u32 c) {
#pragma unroll 1
      for (u32 spins = 0; spins < SPIN_CAP; ++spins) { const v4u f = *(volatile LAS v4u*)(size_t)L_FLG; if ((u32)__builtin_amdgcn_readfirstlane(min(min(f.x, f.y), min(f.z, f.w))) > c) break; }
      asm volatile("" ::: "memory");
    };
    v4u xf[4], ring[5];
    WAIT_VM(11);
    xf[0] = ldsr<v4u>(xa0); xf[1] = ldsr<v4u>(xa1);
    chunk_wait(0);
    sfor<4>([&](auto j) { ring[j] = trfrag(addr(j)); });
    SBAR();
    sfor<96>([&](auto i_) {
      constexpr int i = i_, s = i / 3, t = i % 3, u = s / 2;
      if constexpr (t == 2) acc1[t] = mfma(xf[s % 4], ring[i % 5], acc1[t]); else acc1[t] = mfma(ring[i % 5], xf[s % 4], acc1[t]);
      SBAR();
      if constexpr (i + 4 < 96) { if constexpr ((i + 4) % 12 == 0) chunk_wait((i + 4) / 12); ring[(i + 4) % 5] = trfrag(addr(ic<i + 4>{})); }
      if constexpr (i % 6 == 0 && u + 4 < 17) { SBAR(); issue(u + 4); }
      if constexpr (i % 6 == 1 && u + 1 < 16) {
        asm volatile("s_waitcnt vmcnt(%0)" ::"i"(2 * ((u + 4 < 16 ? u + 4 : 16) - (u + 1))) : "memory");
        xf[(s + 2) % 4] = ldsr<v4u>(xa0 + ((u + 1) % 6) * 2048); xf[(s + 3) % 4] = ldsr<v4u>(xa1 + ((u + 1) % 6) * 2048);
      }
      SBAR();
    });
    WAIT_VM(0);
    fix_tags();
    sfor<4>([&](auto i) { pin(bb[i]); });
    sfor<2>([&](auto f) { const u32 ra = xs + (16 % 6) * 2048 + t32 * 64 + 8 * h, z = ((t32 >> 2) & 3) * 16;
                          const v2u a = ldsr<v2u>(ra + ((2 * f * 16) ^ z)), b = ldsr<v2u>(ra + (((2 * f + 1) * 16) ^ z));
                          res[f] = (v4u){a.x, a.y, b.x, b.y}; b2p[f] = (v4u){bb[2 * f].x, bb[2 * f].y, bb[2 * f + 1].x, bb[2 * f + 1].y}; });
    BARRIER();
    Aux ax; aux_read(ax, false);
    SBAR();
    aux_make(ax, false);
    acc1[1] = mfma(aux_col(ax.c[1]), ax.tf, acc1[1]); acc1[2] = mfma(ax.tf, aux_col(ax.c[2]), acc1[2]); acc1[0] = mfma(aux_col(ax.c[0]), ax.tf, acc1[0]);
    kv_epilogue(WS_KV + (T13 * 16 + m) * 4096, ax, acc1[1], acc1[2]);
    sfor<4>([&](auto i) { ldsw<v4u>(lk + i * 1024, kv[i]); });
    q_epilogue(ax, acc1[0]);
  }
  for (int L = 0;; ++L) {
    IDS();
    const u32 lane = fresh(lane0), l16 = lane * 16, h = lane >> 5, t32 = lane & 31;
    auto res_init = [&](f16v (&acc)[2], bool bias) {
      sfor<8>([&](auto i) { const u32 r = res[i / 4][i % 4], b = bias ? fresh(b2p[i / 4][i % 4]) : 0u;
                            acc[0][2 * i] = bias ? blo(r) + blo(b) : blo(r); acc[0][2 * i + 1] = bias ? bhi(r) + bhi(b) : bhi(r); });
      acc[1] = (f16v){};
      pin(acc[0]);
    };
    auto res_epilogue = [&](f16v (&acc)[2], int buf, bool last) {
      const u32 xa = fresh(L_XCH + w * 4096 + l16), xb = fresh(L_XCH + (w ^ 2) * 4096 + l16);
      const u32 so = sfresh(WS_XF + buf * 2 * MB + (T24 * 32 + cb * 4 + kh * 2) * 1024), ss = sfresh(WS_ST + buf * 256 * KB + (T24 * 16 + cb * 2 + kh) * 256);
      sfor<4>([&](auto g) { ldsw<f4>(xa + g * 1024, (f4){acc[1][4 * g], acc[1][4 * g + 1], acc[1][4 * g + 2], acc[1][4 * g + 3]}); });
      BARRIER();
      f4 o[4]; sfor<4>([&](auto g) { o[g] = ldsr<f4>(xb + g * 1024); });
      f16v v; sfor<16>([&](auto r) { v[(int)r] = acc[0][(int)r] + o[r / 4][r % 4]; });
      res[0] = canon(pk8(v, 0)); res[1] = canon(pk8(v, 8));
      if (!last) {
        bst16<0>(res[0], l16, rws, so); bst16<1024>(res[1], l16, rws, so);
        SBAR();
        float s1 = 0.f, s2 = 0.f;
        sfor<8>([&](auto i) { const u32 eu = res[i / 4][i % 4]; const bf2 e = __builtin_bit_cast(bf2, eu);
                              s1 = __builtin_amdgcn_fdot2_f32_bf16(e, __builtin_bit_cast(bf2, 0x3f803f80u), s1, false); s2 = __builtin_amdgcn_fdot2_f32_bf16(e, e, s2, false); });
        s1 = hsum(s1); s2 = hsum(s2);
        bst8((v2u){canon(fb(s1)), canon(fb(s2))}, t32 * 8, rws, ss);
        if (E2) __builtin_amdgcn_s_sleep(E2 - 1); else WAIT_VM(0);
        BARRIER();
      } else {
        GAS unsigned char* op = (GAS unsigned char*)p.out + (T24 * 32 + t32) * 1024 + (cb * 64 + kh * 32 + 8 * h) * 2;
        sfor<2>([&](auto f) {
          auto a = __builtin_amdgcn_permlane32_swap(res[f].x, res[f].z, false, false), b = __builtin_amdgcn_permlane32_swap(res[f].y, res[f].w, false, false);
          *(GAS v4u*)(op + f * 32) = (v4u){a[0], b[0], a[1], b[1]}; });
      }
    };
    {
      Attn at; at.init(lane, hw); at.q[0] = qf[0]; at.q[1] = qf[1];
      at.diag([&]() { if (L == 0) { kv_flag(0); LBAR();   } });
      BARRIER();
      at.rest([&]() { LBAR();   });
      const u32 so = WS_OF + L * 2 * MB + (T13 * 32 + 2 * m) * 1024; const int jmin = max(8 - T13, 0);
      bst16<0>(at.finish<0>(jmin), l16, rws, so); bst16<1024>(at.finish<1>(jmin), l16, rws, so);
    }
    BARRIER();
    if (E2) __builtin_amdgcn_s_sleep(E2 - 1); else WAIT_VM(0);
    BARRIER();
    {
      constexpr int LOOK = 10;
      v4u win[LOOK];
      f16v acc2[2];
      const u32 so = WS_OF + L * 2 * MB + (T24 * 32 + kh * 16) * 1024, wb = fresh(L_WO + kh * 33 * KB + trbase(lane)), wc = fresh(wb ^ 1024);
      auto issue = [&](auto i, v4u& d) { bld16<(i & 3) * 1024, true>(d, l16, rws, so + (((i >> 2) + rot2) & 3) * 4096); };
      auto addr = [&](auto i) { return ((i & 1) ? wc : wb) + (i / 2) * 2048; };
      sfor<LOOK>([&](auto i) { issue(i, win[i]); });
      res_init(acc2, false);
      gemm<2, 16, LOOK, LOOK, false, true>(acc2, win, issue, addr, [&](auto i) { if constexpr (i == 16) LBAR();   } RETRY_PASS);
      res_epilogue(acc2, 2 * L, false);
    }
    const u32 soh = WS_HF + L * 7 * MB + (T24 * 112 + kh * 56) * 1024;
    if (m < 14) {
      constexpr int LOOK = 22;
      v4u win[LOOK];
      f16v acc3[4] = {};
      const u32 so = WS_XF + 2 * L * 2 * MB + T13 * 32 * 1024, wb0 = fresh(L_W1 + trbase(lane)), wb1 = fresh(wb0 + 63 * KB), wb2 = fresh(wb0 + 126 * KB);
      const u32 sh = WS_HF + L * 7 * MB + (T13 * 112 + m * 8) * 1024;
      auto issue = [&](auto i, v4u& d) { bld16<(i & 3) * 1024, true>(d, l16, rws, so + (((i >> 2) + rot) & 7) * 4096); };
      auto addr = [&](auto i) { constexpr int o = ((i + 60) & 127) * 1024; return o < 63 * KB ? wb0 + o : o < 126 * KB ? wb1 + (o - 63 * KB) : wb2 + (o - 126 * KB); };
      sfor<8>([&](auto i) { issue(i, win[i]); });
      Aux ax;
      constexpr float K1 = -2.0f * 0.7978845608028654f * 1.4426950408889634f, K2 = K1 * 0.044715f;
      float c1, c2, za, zb; v4u q;
      auto gelu_stage = [&](auto e, auto g) {
        const float ua = acc3[e / 8][2 * (e % 8)], ub = acc3[e / 8][2 * (e % 8) + 1], sd = ax.st.y;
        if constexpr (g == 0) { za = __builtin_fmaf(ua * ua, c2, c1); zb = __builtin_fmaf(ub * ub, c2, c1); }
        if constexpr (g == 1) { za = ex2(za * ua); zb = ex2(zb * ub); }
        if constexpr (g == 2) { za = __builtin_amdgcn_rcpf(__builtin_fmaf(za, sd, sd)); zb = __builtin_amdgcn_rcpf(__builtin_fmaf(zb, sd, sd)); }
        if constexpr (g == 3) { za *= ua; zb *= ub; }
        pin(za); pin(zb);
        if constexpr (g == 3) { u32 r = pk(za, zb); pin(r); q[e % 4] = r; if constexpr (e % 4 == 3) bst16<((e / 4) & 3) * 1024>(q, l16, rws, sh); }
      };
      gemm<4, 32, LOOK, LOOK, false, true, 8, 16>(acc3, win, issue, addr, [&](auto i) {
        if constexpr (i == 40) LBAR();
        if constexpr (i == 60) { LBAR(); aux_read(ax, true); }
        if constexpr (i == 66) { aux_make(ax, true); c1 = K1 * ax.rs; c2 = K2 * ax.rs * ax.rs * ax.rs; pin(c1); pin(c2); }
        if constexpr (i >= 70 && i < 74) acc3[i & 3] = mfma(aux_col(ax.c[i & 3]), ax.tf, acc3[i & 3]);
        if constexpr (i >= 80) gelu_stage(ic<(i - 80) / 4>{}, ic<i % 4>{});
      } RETRY_PASS);
      SBAR(); WAIT_VM(1);
      BARRIER();
      sfor<5>([&](auto f_) {
        constexpr int f = 3 + f_;
        sfor<4>([&](auto d) {
          const f2 u = {acc3[f / 2][(f & 1) * 8 + 2 * d], acc3[f / 2][(f & 1) * 8 + 2 * d + 1]}, sd = {ax.st.y, ax.st.y};
          const f2 z = u * __builtin_elementwise_fma(u * u, (f2){c2, c2}, (f2){c1, c1});
          const f2 dn = __builtin_elementwise_fma((f2){ex2(z.x), ex2(z.y)}, sd, sd);
          const f2 g = u * (f2){__builtin_amdgcn_rcpf(dn.x), __builtin_amdgcn_rcpf(dn.y)};
          q[(int)d] = pk(g.x, g.y);
        });
        bst16<(f & 3) * 1024>(q, l16, rws, sh + (f >> 2) * 4096);
        SBAR();
        if constexpr (f == 5) { WAIT_VM(2); LBAR(); }
      });
    } else { LBAR(); LBAR(); BARRIER(); LBAR(); }
    {
      constexpr int LOOK = 12;
      v4u win[LOOK];
      f16v acc2[2];
      const u32 wb0 = fresh(kh * 3072 + trbase(lane)), wb1 = fresh(wb0 + 60 * KB), wb2 = fresh(wb0 + 120 * KB), wc0 = fresh(wb0 ^ 1024), wc1 = fresh(wb1 ^ 1024), wc2 = fresh(wb2 ^ 1024);
      u32 pb[7]; sfor<7>([&](auto j) { const int jj = j + rot4; pb[j] = soh + (jj >= 7 ? jj - 7 : jj) * 8192; });
      const u32 l16b = l16 + 4096;
      auto issue = [&](auto i, v4u& d) { constexpr int k = w2_kstep(i); bld16<(k & 3) * 1024, true>(d, (k & 4) ? l16b : l16, rws, pb[k >> 3]); };
      auto addr = [&](auto i) { constexpr int o = w2_pos(i / 2) * 4096; constexpr bool c = i & 1;
                                return o < 60 * KB ? (c ? wc0 : wb0) + o : o < 120 * KB ? (c ? wc1 : wb1) + (o - 60 * KB) : (c ? wc2 : wb2) + (o - 120 * KB); };
      res_init(acc2, true);
      WAIT_VM(2);
      BARRIER();
      sfor<LOOK>([&](auto i) { issue(i, win[i]); });
      gemm<2, 56, LOOK, LOOK, false, false>(acc2, win, issue, addr, [&](auto i) { if constexpr (i == 0 || i == 8 || i == 36 - DEP || i == 33 || i == 63 || i == 76 - DEP || i == 75 || i == 96) LBAR(); } RETRY_PASS);
      res_epilogue(acc2, 1, L == 1);
    }
    if (L == 1) break;
    {
      constexpr int LOOK = 12;
      const u32 so = WS_XF + 1 * 2 * MB + T13 * 32 * 1024, wb0 = fresh(L_WQ + trbase(lane)), wb1 = fresh(wb0 + 48 * KB);
      auto issue = [&](auto i, v4u& d) { bld16<(i & 3) * 1024, true>(d, l16, rws, so + (((i >> 2) + rot) & 7) * 4096); };
      auto addr = [&](auto i) { constexpr int o = (i / 2) * 3072 + 1024 + (i & 1) * 1024; return o < 48 * KB ? wb0 + o : wb1 + (o - 48 * KB); };
      auto addrq = [&](auto i) { constexpr int o = i * 3072; return o < 48 * KB ? wb0 + o : wb1 + (o - 48 * KB); };
      const u32 sk = sfresh(WS_KV + 4 * MB + (T13 * 16 + m) * 4096);
      f16v acc1[2] = {};
      v4u xq[32];
      sfor<LOOK>([&](auto i) { issue(i, xq[i]); });
      Aux ax;
      gemm<2, 32, LOOK, 32, true, true>(acc1, xq, issue, addr, [&](auto i) {
        if constexpr (i == 40) { LBAR(); aux_read(ax, false); }
        if constexpr (i == 48) aux_make(ax, false);
        if constexpr (i == 52) acc1[0] = mfma(aux_col(ax.c[1]), ax.tf, acc1[0]);
        if constexpr (i == 53) acc1[1] = mfma(ax.tf, aux_col(ax.c[2]), acc1[1]);
      } RETRY_PASS);
      kv_epilogue(sk, ax, acc1[0], acc1[1]);
      f16v accq = mfma(aux_col(ax.c[0]), ax.tf, (f16v){});
      gemm<1, 32, 0, 32, false, false>(&accq, xq, 0, addrq, [&](auto i) {
        if constexpr (i % 4 == 1 && i < 16) ldsw<v4u>(lk + (i / 4) * 1024, kv[i / 4]);
        if constexpr (i == 17) { kv_flag(1); LBAR();   } } RETRY_PASS);
      q_epilogue(ax, accq);
    }
  }
  STAMP(); STAMPCLK(1);
  { IDS(); if (w == 0 && m == 0 && lane0 == 0) bst8(tagv, 0, rws, WS_EPOCH + grp * 256); }
}

extern "C" void kernel_launch(void* const* d_in, const int* in_sizes, int n_in, void* d_out, int out_size, void* d_ws, size_t ws_size, hipStream_t stream) {
  Params p;
  p.x = d_in[0]; p.wqkv = d_in[1]; p.wo = d_in[2]; p.w1 = d_in[4]; p.b1 = d_in[5]; p.w2 = d_in[6]; p.b2 = d_in[7]; p.out = d_out; p.ws = (unsigned char*)d_ws;
  (void)hipFuncSetAttribute((const void*)fwd, hipFuncAttributeMaxDynamicSharedMemorySize, LDS_BYTES);
  hipLaunchKernelGGL(fwd, dim3(256), dim3(512), LDS_BYTES, stream, p);
}
```

```cpp
#include <hip/hip_runtime.h>
#include <type_traits>
#include <utility>

#define KVDELAY 40
#define QSTART 4
#define MAP 1
#define STAMPS 0

#define GAS __attribute__((address_space(1)))
#define LAS __attribute__((address_space(3)))
#define DI __device__ __forceinline__
typedef unsigned u32;
typedef unsigned long long u64;
typedef u32 v4u __attribute__((ext_vector_type(4)));
typedef u32 v2u __attribute__((ext_vector_type(2)));
typedef int v4i __attribute__((ext_vector_type(4)));
typedef short s4 __attribute__((ext_vector_type(4)));
typedef __bf16 bf8 __attribute__((ext_vector_type(8)));
typedef __bf16 bf2 __attribute__((ext_vector_type(2)));
typedef float f2 __attribute__((ext_vector_type(2)));
typedef float f4 __attribute__((ext_vector_type(4)));
typedef float f16v __attribute__((ext_vector_type(16)));
template <int I> using ic = std::integral_constant<int, I>;

template <class F, int... I> DI void sfor_impl(F&& f, std::integer_sequence<int, I...>) { (f(ic<I>{}), ...); }
template <int N, class F> DI void sfor(F&& f) { sfor_impl(f, std::make_integer_sequence<int, N>{}); }

constexpr int KB = 1024, MB = 1024 * 1024;
constexpr int WS_EPOCH = 0;
constexpr int WS_FLAG = 32 * KB;
constexpr int WS_XF = 1 * MB;
constexpr int WS_ST = WS_XF + 6 * MB;
constexpr int WS_KV = WS_ST + 1 * MB;
constexpr int WS_OF = WS_KV + 8 * MB;
constexpr int WS_HF = WS_OF + 4 * MB;
constexpr int WS_STAMP = WS_HF + 14 * MB;
constexpr int WS_END = WS_STAMP + (STAMPS ? MB : 0);
constexpr int L_KV = 0, L_ONES = 48 * KB, L_TA = 54 * KB, L_TB = 58 * KB, L_WQ = 60 * KB, L_WO = 92 * KB, L_W1 = 0, L_XCH = 44 * KB;
constexpr int L_SD = 156 * KB, L_RS = L_SD + 1024, L_CSQ = L_RS + 512, L_CS1 = L_CSQ + 384, L_B1 = L_CS1 + 512, L_ZERO = L_B1 + 512, L_FLG = L_ZERO + 16, LDS_BYTES = 160 * KB;

struct Params { const void *x, *wqkv, *wo, *w1, *b1, *w2, *b2; void* out; unsigned char* ws; };

DI v4i mk_rsrc(const void* p, u32 bytes) { v4i r; r.x = (int)(u32)(u64)p; r.y = (int)((u32)((u64)p >> 32) & 0xffffu); r.z = (int)bytes; r.w = 0x00020000; return r; }
template <int OFF, bool SC1> DI void bld16(v4u& d, u32 voff, v4i rs, u32 soff) {
  if constexpr (SC1) asm volatile("buffer_load_dwordx4 %0, %1, %2, %3 offen offset:%4 sc1" : "=v"(d) : "v"(voff), "s"(rs), "s"(soff), "i"(OFF));
  else asm volatile("buffer_load_dwordx4 %0, %1, %2, %3 offen offset:%4" : "=v"(d) : "v"(voff), "s"(rs), "s"(soff), "i"(OFF));
}
template <int OFF> DI void bld16_rw(v4u& d, u32 voff, v4i rs, u32 soff) { asm volatile("buffer_load_dwordx4 %0, %1, %2, %3 offen offset:%4 sc1" : "+v"(d) : "v"(voff), "s"(rs), "s"(soff), "i"(OFF)); }
template <int OFF, bool SC1> DI void bld8(v2u& d, u32 voff, v4i rs, u32 soff) {
  if constexpr (SC1) asm volatile("buffer_load_dwordx2 %0, %1, %2, %3 offen offset:%4 sc1" : "=v"(d) : "v"(voff), "s"(rs), "s"(soff), "i"(OFF));
  else asm volatile("buffer_load_dwordx2 %0, %1, %2, %3 offen offset:%4" : "=v"(d) : "v"(voff), "s"(rs), "s"(soff), "i"(OFF));
}
template <int OFF> DI void bst16(v4u d, u32 voff, v4i rs, u32 soff) {
  asm volatile("buffer_store_dwordx4 %0, %1, %2, %3 offen offset:%4 sc1\n\ts_nop 1" ::"v"(d), "v"(voff), "s"(rs), "s"(soff), "i"(OFF));
}
DI void bst8(v2u d, u32 voff, v4i rs, u32 soff) { asm volatile("buffer_store_dwordx2 %0, %1, %2, %3 offen sc1" ::"v"(d), "v"(voff), "s"(rs), "s"(soff)); }
DI void touch(u32& d, u32 voff, v4i rs, u32 soff) { asm volatile("buffer_load_dword %0, %1, %2, %3 offen" : "=v"(d) : "v"(voff), "s"(rs), "s"(soff)); }
template <bool SC1 = false> DI void dma16(u32 lds, u32 voff, v4i rs, u32 soff) {
  if constexpr (SC1) asm volatile("s_mov_b32 m0, %0\n\ts_nop 0\n\tbuffer_load_dwordx4 %1, %2, %3 offen sc1 lds" ::"s"(lds), "v"(voff), "s"(rs), "s"(soff));
  else asm volatile("s_mov_b32 m0, %0\n\ts_nop 0\n\tbuffer_load_dwordx4 %1, %2, %3 offen lds" ::"s"(lds), "v"(voff), "s"(rs), "s"(soff));
}
template <int N, class T> DI void wait_vm(T& x) { asm volatile("s_waitcnt vmcnt(%1)" : "+v"(x) : "i"(N)); }
template <int N, class T> DI void wait_vm(T& x, T& y) { asm volatile("s_waitcnt vmcnt(%2)" : "+v"(x), "+v"(y) : "i"(N)); }
template <class T> DI void pin(T& x) { asm volatile("" : "+v"(x)); }
#define WAIT_VM(n) asm volatile("s_waitcnt vmcnt(" #n ")" ::: "memory")
#define BARRIER() do { SBAR(); if (STAMPS != 3) STAMP(); asm volatile("s_waitcnt lgkmcnt(0)\n\ts_barrier" ::: "memory"); SBAR(); } while (0)
#define STAMPL 0
#define LBAR() do { SBAR(); if (STAMPL) STAMP(); asm volatile("s_barrier" ::: "memory"); SBAR(); } while (0)
#define HBAR() do { if (STAMPL) STAMP(); asm volatile("s_waitcnt lgkmcnt(0)\n\ts_barrier" ::: "memory"); } while (0)
#define SBAR() __builtin_amdgcn_sched_barrier(0)
DI f16v mfma(v4u a, v4u b, f16v c) { return __builtin_amdgcn_mfma_f32_32x32x16_bf16(__builtin_bit_cast(bf8, a), __builtin_bit_cast(bf8, b), c, 0, 0, 0); }
DI u32 pk(float lo, float hi) { bf2 r = __builtin_convertvector((f2){lo, hi}, bf2); return __builtin_bit_cast(u32, r); }
DI float blo(u32 v) { return __builtin_bit_cast(float, v << 16); }
DI float bhi(u32 v) { return __builtin_bit_cast(float, v & 0xffff0000u); }
template <class T> DI T ldsr(u32 a) { return *(LAS T*)(size_t)a; }
template <class T> DI void ldsw(u32 a, T v) { *(LAS T*)(size_t)a = v; }
DI v4u trfrag(u32 a) {
  const v2u lo = __builtin_bit_cast(v2u, __builtin_amdgcn_ds_read_tr16_b64_v4i16((LAS s4*)(size_t)a));
  const v2u hi = __builtin_bit_cast(v2u, __builtin_amdgcn_ds_read_tr16_b64_v4i16((LAS s4*)(size_t)(a + 512)));
  return (v4u){lo.x, lo.y, hi.x, hi.y};
}
DI f2 halves(float v) {
  auto r = __builtin_amdgcn_permlane32_swap(__builtin_bit_cast(u32, v), __builtin_bit_cast(u32, v), false, false);
  const u32 a = r[0], b = r[1]; return (f2){__builtin_bit_cast(float, a), __builtin_bit_cast(float, b)};
}
DI float bf(u32 v) { return __builtin_bit_cast(float, v); }
DI u32 fb(float v) { return __builtin_bit_cast(u32, v); }
DI float fadd(float a, float b) { float r; asm("v_add_f32 %0, %1, %2" : "=v"(r) : "v"(a), "v"(b)); return r; }
DI float hsum(float v) { const f2 r = halves(v); return r.x + r.y; }
DI float hmax(float v) { const f2 r = halves(v); return __builtin_fmaxf(r.x, r.y); }
DI float mx3(float a, float b, float c) { return __builtin_fmaxf(__builtin_fmaxf(a, b), c); }
DI float max16(const f16v& s, float m) {
  float a = mx3(m, s[0], s[1]), b = mx3(s[2], s[3], s[4]);
  a = mx3(a, s[5], s[6]); b = mx3(b, s[7], s[8]); a = mx3(a, s[9], s[10]); b = mx3(b, s[11], s[12]); a = mx3(a, s[13], s[14]);
  return mx3(a, b, s[15]);
}
DI v4u pk8(const f16v& v, int o) { return (v4u){pk(v[o], v[o + 1]), pk(v[o + 2], v[o + 3]), pk(v[o + 4], v[o + 5]), pk(v[o + 6], v[o + 7])}; }
#define SPIN_LOG 12
constexpr u32 SPIN_CAP = 1u << SPIN_LOG;
DI u32 fresh(u32 v) { asm volatile("" : "+v"(v)); return v; }
DI u32 sfresh(u32 v) { asm volatile("" : "+s"(v)); return v; }
constexpr u32 EMPTY = 0xffffffffu;
DI u32 umax3(u32 a, u32 b, u32 c) { return max(max(a, b), c); }
DI u32 canon(u32 v) { return min(v, EMPTY - 1); }
DI v4u canon(v4u v) { return (v4u){canon(v.x), canon(v.y), canon(v.z), canon(v.w)}; }
DI bool empty2(const v4u& a, const v4u& b) { return __any(max(umax3(a.x, a.y, a.z), umax3(umax3(a.w, b.x, b.y), b.z, b.w)) == EMPTY); }

#define RETRY_ARG
#define RETRY_PASS
#define RETRY_INC()
template <int P, int N, int NX> DI u32 validate1(v4u (&x)[NX]) {
  v4u &a = x[P % NX], &b = x[(P + 1) % NX];
  wait_vm<N>(a, b);
  u32 v = umax3(umax3(a.x, a.y, a.z), a.w, b.x); pin(v); return v;
}
template <int P, int NX> DI u64 validate2(u32 v, v4u (&x)[NX]) { const v4u& b = x[(P + 1) % NX]; u64 e = __ballot(max(umax3(v, b.y, b.z), b.w) == EMPTY); asm volatile("" : "+s"(e)); return e; }
template <int P, int NX, class FI> DI void validate3(u64 e, v4u (&x)[NX], FI&& issue RETRY_ARG) {
  v4u &a = x[P % NX], &b = x[(P + 1) % NX];
  if (__builtin_expect(e != 0, 0)) {
#pragma unroll 1
    for (u32 spins = 0; spins < SPIN_CAP; ++spins) {
      __builtin_amdgcn_s_sleep(4); RETRY_INC();
      issue(ic<P>{}, a); issue(ic<P + 1>{}, b);
      wait_vm<0>(a, b);
      if (!empty2(a, b)) break;
    }
  }
}
template <int N> DI void wait_lgkm() { __builtin_amdgcn_s_waitcnt(0xc07f | (N << 8)); }
constexpr int cmin(int a, int b) { return a < b ? a : b; }
constexpr int issued(int s, int KS, int LOOK, int INIT) { return s == 0 ? INIT : cmin(cmin(INIT + 2 * s, ((s - 1) & ~1) + LOOK), KS); }
constexpr int DEP = 5;
template <int NT, int KS, int KM> constexpr int ord_s(int i) { return i < KM * NT ? i / NT : KM + (i - KM * NT) % (KS > KM ? KS - KM : 1); }
template <int NT, int KS, int KM> constexpr int ord_t(int i) { return i < KM * NT ? i % NT : (i - KM * NT) / (KS > KM ? KS - KM : 1); }
template <int NT, int KS, int LOOK, int NX, bool SWAPLAST, bool VAL, int INIT = LOOK, int KM = KS, class FI, class FA, class FH>
DI void gemm(f16v* acc, v4u (&x)[NX], FI&& issue, FA&& addr, FH&& hook RETRY_ARG) {
  constexpr int N1 = KS * NT;
  v4u ring[DEP + 1];
  u32 vv; u64 ve;
  auto unit = [&](auto i) { return addr(ic<ord_s<NT, KS, KM>(i) * NT + ord_t<NT, KS, KM>(i)>{}); };
  sfor<DEP>([&](auto i) { ring[i] = trfrag(unit(i)); });
  SBAR();
  if constexpr (LOOK > 0 && VAL) { vv = validate1<0, INIT - 2>(x); ve = validate2<0>(vv, x); validate3<0>(ve, x, issue RETRY_PASS); }
  if constexpr (LOOK > 0 && !VAL) wait_vm<INIT - 2>(x[0], x[1]);
  sfor<N1>([&](auto i_) {
    constexpr int i = i_, s = ord_s<NT, KS, KM>(i), t = ord_t<NT, KS, KM>(i);
    if constexpr ((i & 1) == 0 && i + DEP <= N1) { wait_lgkm<2 * DEP - 4>(); SBAR(); }
    if constexpr (SWAPLAST && t == NT - 1) acc[t] = mfma(x[s % NX], ring[i % (DEP + 1)], acc[t]);
    else acc[t] = mfma(ring[i % (DEP + 1)], x[s % NX], acc[t]);
    SBAR();
    hook(i_);
    SBAR();
    if constexpr (i + DEP < N1) ring[(i + DEP) % (DEP + 1)] = trfrag(unit(ic<i + DEP>{}));
    if constexpr (LOOK > 0 && i < KM * NT) {
      constexpr int f0 = issued(s, KS, LOOK, INIT), f1 = issued(s + 1, KS, LOOK, INIT);
      static_assert(NT > 2 || (s & 1) == 0 || f1 == f0, "two-tile loops issue in even steps only");
      if constexpr (t < 2 && f0 + t < f1) { SBAR(); issue(ic<f0 + t>{}, x[(f0 + t) % NX]); }
      constexpr int G = KM == KS ? 2 * NT : NT, g = i % G, P = (i / G + 1) * 2;
      static_assert(KM == KS || 2 * KM == KS, "tail = half of the steps");
      if constexpr (P < KS) {
        if constexpr (VAL) {
          if constexpr (g == G - 3) { SBAR(); vv = validate1<P, f1 - (P + 2)>(x); }
          if constexpr (g == G - 2) { SBAR(); ve = validate2<P>(vv, x); }
          if constexpr (g == G - 1) { SBAR(); validate3<P>(ve, x, issue RETRY_PASS); }
        } else if constexpr (g == G - 1) { SBAR(); wait_vm<f1 - (P + 2)>(x[P % NX], x[(P + 1) % NX]); }
      }
    }
    SBAR();
  });
}

#define STAMP() do {} while (0)
#define STAMPCLK(i) do {} while (0)
DI f2 ln_stats(float s1, float s2) {
  const float mean = s1 * (1.0f / 512.0f), var = __builtin_fmaxf(s2 * (1.0f / 512.0f) - mean * mean, 0.0f);
  return (f2){-mean, __builtin_amdgcn_sqrtf(var + 1e-5f)};
}
DI u32 trbase(u32 lane) { return (4 * (lane >> 5) + ((lane & 15) >> 2)) * 64 + ((lane >> 4) & 1) * 32 + (lane & 3) * 8; }
DI float ex2(float x) { return __builtin_amdgcn_exp2f(x); }
DI v4u expk8(const f16v& s, int o) { return (v4u){pk(ex2(s[o]), ex2(s[o + 1])), pk(ex2(s[o + 2]), ex2(s[o + 3])), pk(ex2(s[o + 4]), ex2(s[o + 5])), pk(ex2(s[o + 6]), ex2(s[o + 7]))}; }
DI v4u aux_tok(f2 st, u32 h) {
  const float v = h ? st.y : st.x; const u32 a = pk(v, v) & 0xffffu; const float r = v - blo(a); const u32 b = pk(r, r) & 0xffffu; const float r2 = r - blo(b);
  return (v4u){a | (b << 16), h ? pk(r2, 0.0f) : a, 0u, 0u};
}
DI v4u aux_col(u32 v) { return (v4u){(v & 0xffffu) | (v << 16), v >> 16, 0u, 0u}; }

struct Attn {
  v4u q[2], kf, v0, v1, p0, p1, t0, t1; f16v o[2], nm[2], sc[2]; u32 ka, va, vb, vstr, l16;
  DI void init(u32 lane, int qb) {
    l16 = lane * 16;
    const bool real = (lane & 16) == 0;
    ka = L_KV + qb * 4096 + l16; vstr = real ? 4096u : 512u;
    va = real ? L_KV + qb * 4096 + 2048 + l16 : L_ONES + qb * 512 + (lane & 15) * 16; vb = real ? va + 1024 : va;
  }
  DI f16v addmask() { f16v c; sfor<4>([&](auto g) { const f4 t = ldsr<f4>(L_TA + g * 1024 + l16); sfor<4>([&](auto i) { c[4 * g + i] = t[(int)i]; }); }); return c; }
  template <int D, int MASK, bool QK, bool KN, int PV> DI void stage(u32 a0, u32 a1, u32 kn) {
    const f16v& si = sc[D];
    float e[16];
    sfor<4>([&](auto i) { e[i] = ex2(si[(int)i]); });
    SBAR();
    wait_lgkm<0>();
    if constexpr (QK) sc[1 - D] = mfma(kf, q[1 - D], nm[1 - D]);
    SBAR();
    if constexpr (KN) kf = ldsr<v4u>(kn);
    sfor<4>([&](auto i) { e[4 + i] = ex2(si[4 + i]); });
    SBAR();
    if constexpr (PV == 1) o[1 - D] = mfma(v0, p0, o[1 - D]);
    if constexpr (PV == 2) o[1 - D] = mfma(v0, p0, (f16v){});
    SBAR();
    v0 = ldsr<v4u>(a0);
    sfor<4>([&](auto i) { e[8 + i] = ex2(si[8 + i]); });
    SBAR();
    if constexpr (PV) o[1 - D] = mfma(v1, p1, o[1 - D]);
    SBAR();
    v1 = ldsr<v4u>(a1);
    sfor<4>([&](auto i) { e[12 + i] = ex2(si[12 + i]); });
    SBAR();
    sfor<4>([&](auto i) { p0[(int)i] = pk(e[2 * i], e[2 * i + 1]); });
    sfor<4>([&](auto i) { p1[(int)i] = pk(e[8 + 2 * i], e[9 + 2 * i]); });
    if constexpr (MASK == 1) { pin(t0); pin(t1); p0 &= ~t0; p1 &= ~t1; }
    if constexpr (MASK == 2) { p0 &= t0; p1 &= t1; }
    pin(p0); pin(p1);
    SBAR();
  }
  DI void diag() {
    const f16v c = addmask();
    v4u kd[2];
    sfor<2>([&](auto d) { kd[d] = ldsr<v4u>(ka + 8 * 4096 + d * 1024); });
    t0 = ldsr<v4u>(L_TB + l16); t1 = ldsr<v4u>(L_TB + 1024 + l16);
    SBAR();
    sfor<2>([&](auto d) { sc[d] = mfma(kd[d], q[d], c); });
    va = fresh(va + 8 * vstr); vb = fresh(vb + 8 * vstr);
    SBAR();
    sfor<2>([&](auto d) {
      const float mx = hmax(max16(sc[d], sc[d][0]));
      sfor<16>([&](auto r) { nm[d][(int)r] = -mx; });
      pin(nm[d]);
      sc[d] = mfma(kd[d], q[d], nm[d]);
      SBAR();
    });
    stage<0, 1, false, false, 0>(va, vb, 0);
    stage<1, 1, false, false, 2>(va + 256, vb + 256, 0);
  }
  template <class F> DI void rest(F&& hint) {
    sc[0] = mfma(ldsr<v4u>(ka + 7 * 4096), q[0], nm[0]); kf = ldsr<v4u>(ka + 7 * 4096 + 1024);
    sfor<8>([&](auto i) {
      constexpr int j = 7 - i;
      va = fresh(va - vstr); vb = fresh(vb - vstr);
      if constexpr (j == 0) hint();
      stage<0, j == 0 ? 2 : 0, true, (j > 0), j == 7 ? 2 : 1>(va, vb, ka + (j - 1) * 4096);
      stage<1, j == 0 ? 2 : 0, (j > 0), (j > 0), 1>(va + 256, vb + 256, ka + (j - 1) * 4096 + 1024);
    });
    o[1] = mfma(v0, p0, o[1]); o[1] = mfma(v1, p1, o[1]);
  }
  template <int D> DI v4u finish(int jmin) {
    f16v& oo = o[D];
    float big = __builtin_fabsf(oo[0]); sfor<7>([&](auto r) { big += __builtin_fabsf(oo[r + 1]); });
    if (__builtin_expect(!__all(oo[8] <= 0x1p60f && big <= 0x1p126f), 0)) {
      auto scores = [&](int j) {
        f16v c = addmask();
        if (j != 8) sfor<16>([&](auto r) { c[(int)r] = (j == 0 && c[(int)r] == 0.0f) ? -__builtin_inff() : 0.0f; });
        return mfma(ldsr<v4u>(ka + j * 4096 + D * 1024), q[D], c);
      };
      float mx = -__builtin_inff();
#pragma unroll 1
      for (int j = jmin; j <= 8; ++j) mx = max16(scores(j), mx);
      mx = hmax(mx);
      oo = (f16v){};
#pragma unroll 1
      for (int j = jmin; j <= 8; ++j) {
        const f16v s = scores(j) - mx;
        oo = mfma(ldsr<v4u>(va + j * vstr + D * 256), expk8(s, 0), oo); oo = mfma(ldsr<v4u>(vb + j * vstr + D * 256), expk8(s, 8), oo);
      }
    }
    const float rl = __builtin_amdgcn_rcpf(oo[8]);
    sfor<8>([&](auto r) { oo[(int)r] *= rl; });
    return canon(pk8(oo, 0));
  }
};

constexpr int w2_kstep(int i) { return ((i % 14) / 2) * 8 + 2 * (i / 14) + (i & 1); }
constexpr int w2r_slot(int i) { return i < 6 ? 6 + 2 * i : 38 + 2 * (i - 6); }
constexpr int w2_pos(int i) { return i < 38 ? (i + 32) % 38 : (i - 6) % 39; }

__global__ void __launch_bounds__(512, 2) fwd(Params p) {
  extern __shared__ __attribute__((aligned(16))) unsigned char smem[];
  const int w0 = __builtin_amdgcn_readfirstlane(threadIdx.x >> 6);
#define IDS() const int w = sfresh(w0), bid = sfresh(blockIdx.x), grp = (bid & 6) * 2 + (bid >> 6), m = (bid & 1) * 8 + ((bid >> 3) & 7), cu = grp * 16 + m, hw = w & 3, cb = (m >> 3) * 4 + (m & 3), th = (m >> 2) & 1, \
              rt = w & 1, kh = (w >> 1) & 1, T13 = 4 * grp + hw, T24 = 4 * grp + 2 * th + rt, rot = (4 * (m & 1) + (grp & 3) + ((m >> 1) & 3)) & 7, rot2 = (2 * th + grp + cb) & 3, rot4 = (2 * (cb & 3) + 4 * th + (grp & 3)) % 7; (void)rot; (void)rot2; (void)rot4; (void)cu; (void)cb; (void)kh; (void)T13; (void)T24
  const v4i rws = mk_rsrc(p.ws, WS_END);
  const u32 lane0 = threadIdx.x & 63;
  STAMP(); STAMPCLK(0);
  if (threadIdx.x < 4) ldsw<u32>(L_FLG + threadIdx.x * 4, 0u);
  BARRIER();
  v2u tagv;
  auto load_tags = [&]() { IDS(); const u32 g = grp + (lane0 < 3 ? lane0 : 0); bld8<0, true>(tagv, (g < 16 ? g : grp) * 256, rws, WS_EPOCH); };
  auto fix_tags = [&]() { pin(tagv); tagv = __builtin_bit_cast(v2u, __builtin_bit_cast(u64, tagv) + 1); };
  auto tag0 = [&]() { return (v2u){(u32)__builtin_amdgcn_readfirstlane(tagv.x), (u32)__builtin_amdgcn_readfirstlane(tagv.y)}; };
  auto publish = [&](int sid) { IDS(); const u32 lane = fresh(lane0); if (lane < 16) bst8(tag0(), lane * 128 + m * 8, rws, WS_FLAG + (sid * 256 + grp * 16) * 128); };
  auto await = [&](int sid, u32 first, u32 mask) {
    IDS(); const u64 t0 = __builtin_bit_cast(u64, tag0()); const u32 vo = (fresh(lane0) & mask) * 8;
#pragma unroll 1
    for (u32 spins = 0; spins < SPIN_CAP; ++spins) {
      v2u f; bld8<0, true>(f, vo, rws, WS_FLAG + (sid * 256 + cu) * 128 + first * 8); wait_vm<0>(f);
      if (__all(__builtin_bit_cast(u64, f) == t0)) break;
      __builtin_amdgcn_s_sleep(2);
    }
  };
  auto flg_wait = [&](bool q, u32 c) {
#pragma unroll 1
    for (u32 spins = 0; spins < SPIN_CAP; ++spins) { const v4u f = *(volatile LAS v4u*)(size_t)L_FLG; if ((u32)__builtin_amdgcn_readfirstlane(q ? f.x : min(f.y, f.z)) > c) break; if (w0 >= 4) __builtin_amdgcn_s_sleep(2); }
    asm volatile("" ::: "memory");
  };
  const u32 wrow = lane0 >> 2, wch = (lane0 & 3) * 16;
  if (w0 >= 4) {
    const v4i ro = mk_rsrc(p.wo, 512 * KB), r1 = mk_rsrc(p.w1, 1792 * KB), r2 = mk_rsrc(p.w2, 1792 * KB);
    u32 vo24, vo1;
    { IDS(); vo24 = wrow * 1024 + wch + cb * 128; vo1 = wrow * 3584 + wch + m * 256; }
    auto stage_w1 = [&](int s0, int s1) {
      IDS();
      if (m >= 14) return;
#pragma unroll 1
      for (int s = s0 + hw; s < s1; s += 4) {
        const u32 so = ((s + 17 + 4 * rot) & 31) * 16 * 3584;
        dma16(L_W1 + s * 4096, vo1, r1, so); dma16(L_W1 + s * 4096 + 1024, vo1, r1, so + 64); dma16(L_W1 + s * 4096 + 2048, vo1, r1, so + 128); dma16(L_W1 + s * 4096 + 3072, vo1, r1, so + 192);
      }
    };
    auto w2_row = [&](int i, int rot4) { const int t = (i * 37) >> 9, r = i - 14 * t, j = (r >> 1) + rot4; return (u32)(((j >= 7 ? j - 7 : j) * 8 + 2 * t + (r & 1)) * 16384); };
    auto stage_w2 = [&](int i0, int i1, int p0) {
      IDS();
#pragma unroll 1
      for (int i = i0 + hw; i < i1; i += 4) {
        const u32 la = (i + p0) * 4096, so = w2_row(i, rot4);
        dma16(la, vo24, r2, so); dma16(la + 1024, vo24, r2, so + 64); dma16(la + 2048, vo24, r2, so + 56 * 16384); dma16(la + 3072, vo24, r2, so + 56 * 16384 + 64);
      }
    };
    const v4u ones = {0x3f803f80u, 0x3f803f80u, 0x3f803f80u, 0x3f803f80u}, empt = {EMPTY, EMPTY, EMPTY, EMPTY};
    auto tables = [&]() {
      IDS(); const u32 lane = fresh(lane0), l16 = lane * 16; const int dq = (int)(lane & 31) - 4 * (int)(lane >> 5);
      sfor<4>([&](auto g) { f4 t; sfor<4>([&](auto i) { t[(int)i] = 8 * g + i <= dq ? 0.0f : -__builtin_inff(); }); ldsw<f4>(L_TA + g * 1024 + l16, t); });
      sfor<2>([&](auto g) { v4u b; sfor<4>([&](auto d) { const int c = 16 * g + 8 * (d >> 1) + 2 * (d & 1); b[(int)d] = (c > dq ? 0xffffu : 0u) | (c + 1 > dq ? 0xffff0000u : 0u); });
                            ldsw<v4u>(L_TB + g * 1024 + l16, b); });
      sfor<6>([&](auto g) { ldsw<v4u>(L_ONES + g * 1024 + l16, grp + g / 2 >= 2 ? ones : (v4u){}); });
    };
    auto cs_write = [&](const f16v& a, u32 dst) {
      const u32 lane = fresh(lane0);
      if ((lane & 31) == 0) sfor<4>([&](auto g) { v4u q; sfor<4>([&](auto i) { const float v = a[4 * g + i]; const u32 hi = pk(v, v) & 0xffffu; q[(int)i] = hi | (pk(0.0f, v - blo(hi)) & 0xffff0000u); });
                                                  ldsw<v4u>(dst + (8 * g + 4 * (lane >> 5)) * 4, q); });
    };
    auto put_stats = [&](f2 a, f2 b, u32 tok2) { ldsw<f4>(L_SD + tok2 * 16, (f4){a.x, a.y, b.x, b.y}); ldsw<f2>(L_RS + tok2 * 8, (f2){__builtin_amdgcn_rcpf(a.y), __builtin_amdgcn_rcpf(b.y)}); };
    v4u keep[24], w2r[30];
    {
      IDS();
      const u32 l16 = lane0 * 16;
      if (hw < 3) {
        const v4i rq = mk_rsrc(p.wqkv, 1536 * KB);
        const u32 vo = (8 * ((wrow >> 2) & 1) + 4 * (wrow >> 3) + (wrow & 3)) * 3072 + wch + m * 64 + hw * 1024;
        auto chunk = [&](int c) { const u32 so = 64 * ((c + rot) & 7) * 3072, la = L_WQ + c * 12288 + hw * 1024; sfor<4>([&](auto i) { dma16(la + i * 3072, vo, rq, so + i * 16 * 3072); }); };
        if (hw == 0) flg_wait(false, QSTART);
        __builtin_amdgcn_s_setprio(3);
        load_tags();
        chunk(0);
        if (hw != 0) __builtin_amdgcn_s_sleep(KVDELAY);
        chunk(1); chunk(2); chunk(3);
        f16v a = {}; const u32 b0 = L_WQ + hw * 1024 + trbase(lane0);
        auto landed = [&](int c) {
          ldsw<u32>(L_FLG + hw * 4, c + 1);
          v4u q[4]; sfor<4>([&](auto i) { q[i] = trfrag(b0 + (4 * c + i) * 3072); }); SBAR();
          sfor<4>([&](auto i) { a = mfma(q[i], ones, a); });
        };
#pragma unroll 1
        for (int c = 0; c < 4; ++c) { WAIT_VM(12); ldsw<u32>(L_FLG + hw * 4, c + 1); chunk(c + 4); landed(c); }
        WAIT_VM(12); landed(4); WAIT_VM(8); landed(5); WAIT_VM(4); landed(6); WAIT_VM(0); landed(7);
        cs_write(a, L_CSQ + hw * 128);
        asm volatile("s_waitcnt lgkmcnt(0)" ::: "memory");
        ldsw<u32>(L_FLG + hw * 4, 9);
        __builtin_amdgcn_s_setprio(0);
        fix_tags();
        if (hw == 1 && m < 14) { const u32 v = ((const GAS u32*)p.b1)[m * 64 + lane0]; ldsw<v2u>(L_B1 + lane0 * 8, (v2u){(v & 0xffffu) | (v << 16), (v >> 16) | (v & 0xffff0000u)}); }
      } else {
        u32 dd[4];
        touch(dd[0], lane0 < 48 ? lane0 * 128 : 0x7fffffffu, mk_rsrc(p.wqkv, 1536 * KB), bid * 6144);
        touch(dd[1], lane0 * 128, mk_rsrc(p.x, 2 * MB), (grp * 128 + m * 8) * 1024);
        load_tags();
        tables();
        WAIT_VM(0); fix_tags(); pin(dd[0]); pin(dd[1]);
        flg_wait(false, 5);
        touch(dd[1], lane0 < 56 ? lane0 * 128 : 0x7fffffffu, r1, bid * 7168); touch(dd[3], lane0 < 16 ? lane0 * 128 : 0x7fffffffu, ro, bid * 2048); touch(dd[2], lane0 < 56 ? lane0 * 128 : 0x7fffffffu, r2, bid * 7168);
        WAIT_VM(0); pin(dd[1]); pin(dd[2]); pin(dd[3]);
      }
      if (STAMPL) { STAMP(); STAMP(); }
      flg_wait(false, 7); flg_wait(true, 7);
    }
    sfor<2>([&](auto L_) {
      constexpr int L = L_;
      IDS();
      const u32 lane = fresh(lane0), l16 = lane * 16;
      auto gather_stats = [&](int buf) {
        __builtin_amdgcn_s_setprio(3);
        v4u q[4]; const u32 so = WS_ST + buf * 256 * KB + T13 * 4096;
#pragma unroll 1
        for (u32 spins = 0; spins < SPIN_CAP; ++spins) {
          bld16<0, true>(q[0], l16, rws, so); bld16<1024, true>(q[1], l16, rws, so); bld16<2048, true>(q[2], l16, rws, so); bld16<3072, true>(q[3], l16, rws, so);
          wait_vm<0>(q[0], q[1]); wait_vm<0>(q[2], q[3]);
          if (!empty2(q[0], q[1]) && !empty2(q[2], q[3])) break;
          __builtin_amdgcn_s_sleep(4);
        }
        float s[4];
        sfor<4>([&](auto i) { const u32 a = q[0][(int)i], b = q[1][(int)i], c = q[2][(int)i], d = q[3][(int)i];
                              float v = fadd(fadd(bf(a), bf(b)), fadd(bf(c), bf(d)));
                              auto r = __builtin_amdgcn_permlane16_swap(fb(v), fb(v), false, false); const u32 r0 = r[0], r1 = r[1];
                              const f2 hh = halves(fadd(bf(r0), bf(r1))); s[i] = fadd(hh.x, hh.y); });
        if (lane < 16) put_stats(ln_stats(s[0], s[1]), ln_stats(s[2], s[3]), hw * 16 + lane);
        __builtin_amdgcn_s_setprio(0);
      };
      auto fill_x = [&](int buf) { const u32 so = WS_XF + buf * 2 * MB + (T24 * 32 + cb * 4 + kh * 2) * 1024; bst16<0>(empt, l16, rws, so); bst16<1024>(empt, l16, rws, so);
                                   if (lane < 16) bst16<0>(empt, l16, rws, WS_ST + buf * 256 * KB + (T24 * 16 + cb * 2 + kh) * 256); };
      auto fill_h = [&](auto i0, auto n) { if (m < 14) sfor<n>([&](auto j) { constexpr int i = i0 + j; bst16<(i & 3) * 1024>(empt, l16, rws, WS_HF + 7 * MB + (T13 * 112 + m * 8 + (i & 4)) * 1024); }); };
      auto stage_wo = [&](int s0, int s1) {
#pragma unroll 1
        for (int s = s0 + hw; s < s1; s += 4) { const u32 so = ((s & 16) + ((s + 4 * rot2) & 15)) * 16384; dma16(L_WO + s * 2048, vo24, ro, so); dma16(L_WO + s * 2048 + 1024, vo24, ro, so + 64); } };
      auto w2_get = [&](v4u* r, int s0) { const u32 so = w2_row(s0, rot4) + (hw & 1) * 16384 + (hw >> 1) * 56 * 16384; bld16<0, false>(r[0], vo24, r2, so); bld16<64, false>(r[1], vo24, r2, so); };
      auto w2_set = [&](v4u* r, int s0, u32 ta) { pin(r[0]); pin(r[1]); const u32 o = ta + ((hw & 1) ? w2_pos(s0 + 1) : w2_pos(s0)) * 4096; ldsw<v4u>(o, r[0]); ldsw<v4u>(o + 1024, r[1]); };
      auto w2_regs = [&](auto i0, auto n) { sfor<n>([&](auto j) { constexpr int i = i0 + j; w2_get(&w2r[2 * i], w2r_slot(i)); }); };
      auto w2_put = [&](auto i0, auto n) { const u32 ta = fresh(l16 + (hw >> 1) * 2048); sfor<n>([&](auto j) { constexpr int i = i0 + j; w2_set(&w2r[2 * i], w2r_slot(i), ta); }); };
      v4u st1[24];
      auto w1_pos = [&](int i) { const int j = hw + 4 * i; return j < 11 ? j : j == 11 ? 23 : j < 16 ? j - 1 : j + 8; };
      auto w1_get = [&](auto i0, auto n) { if (m < 14) sfor<n>([&](auto j) { constexpr int i = i0 + j; const u32 so = ((w1_pos(i) + 17 + 4 * rot) & 31) * 16 * 3584;
                                                                             sfor<4>([&](auto t) { bld16<t * 64, false>(st1[4 * i + t], vo1, r1, so); }); }); };
      auto w1_put = [&](auto i0, auto n) { if (m < 14) sfor<n>([&](auto j) { constexpr int i = i0 + j; const u32 o = fresh(l16) + L_W1 + w1_pos(i) * 4096;
                                                                             sfor<4>([&](auto t) { pin(st1[4 * i + t]); ldsw<v4u>(o + t * 1024, st1[4 * i + t]); }); }); };
      f16v cs = {};
      auto w1_colsum = [&](int p0, int p1) {
        const u32 b0 = L_W1 + hw * 1024 + trbase(lane);
#pragma unroll 1
        for (int z = p0; z < p1; z += 4) { v4u q[4]; sfor<4>([&](auto i) { q[i] = trfrag(b0 + (z + i < p1 ? z + i : p0) * 4096); }); SBAR();
                                           sfor<4>([&](auto i) { cs = mfma(q[i], z + i < p1 ? ones : (v4u){}, cs); }); }
      };
      auto halo = [&]() {
        __builtin_amdgcn_s_setprio(3);
        if (4 * grp + 2 * hw >= 8) {
          await(L, 2 * hw, 1);
          sfor<8>([&](auto i) { dma16<true>(L_KV + hw * 8192 + i * 1024, l16, rws, WS_KV + L * 4 * MB + ((4 * grp - 8 + 2 * hw + (i >> 2)) * 16 + m) * 4096 + (i & 3) * 1024); });
        } else sfor<8>([&](auto i) { ldsw<v4u>(L_KV + hw * 8192 + i * 1024 + l16, (v4u){}); });
        __builtin_amdgcn_s_setprio(0);
      };
      auto hint = [&](int sid) { if (hw == 3) { __builtin_amdgcn_s_setprio(3); publish(sid); __builtin_amdgcn_s_setprio(0); } };
      v2u hf;
      auto ask = [&](int sid) { if (hw == 2) bld8<0, true>(hf, (lane & 15) * 8, rws, WS_FLAG + (sid * 256 + cu) * 128); };
      auto hinted = [&](int sid) { if (hw == 2) { __builtin_amdgcn_s_setprio(3); wait_vm<0>(hf); if (!__all(__builtin_bit_cast(u64, hf) == __builtin_bit_cast(u64, tag0()))) await(sid, 0, 15); __builtin_amdgcn_s_setprio(0); } };
      if constexpr (L == 1) HBAR();
      if (L == 0) sfor<24>([&](auto i) { keep[i] = ldsr<v4u>(L_WQ + hw * 24 * KB + i * 1024 + l16); });
      else if (hw == 3) tables();
      w1_get(ic<3>{}, ic<3>{});
      HBAR();
      if (L == 1) fill_h(ic<3>{}, ic<2>{});
      __builtin_amdgcn_s_sleep(6);
      halo();
      fill_x(2 * L);
      WAIT_VM(3); BARRIER();
      if (L == 0) { const u32 so = WS_OF + (T13 * 32 + 2 * m) * 1024; bst16<0>(empt, l16, rws, so); bst16<1024>(empt, l16, rws, so); }
      stage_wo(0, 32);
      stage_w1(15, 23);
      w1_get(ic<0>{}, ic<3>{});
      WAIT_VM(0);
      HBAR();
      hint(3 + L);
      if (L == 0) w2_regs(ic<0>{}, ic<4>{}); else fill_h(ic<5>{}, ic<3>{});
      BARRIER();
      ask(3 + L);
      w1_put(ic<0>{}, ic<2>{});
      if (hw < 3) w1_put(ic<2>{}, ic<1>{});
      hinted(3 + L);
      BARRIER();
      if (L == 0 && m < 14) w1_colsum(0, 11);
      if (L == 1) WAIT_VM(0);
      HBAR();
      hint(5 + L);
      if (L == 0 && m < 14) w1_colsum(15, 19);
      BARRIER();
      ask(5 + L);
      w1_put(ic<4>{}, ic<2>{});
      if (hw == 3) w1_put(ic<2>{}, ic<1>{});
      hinted(5 + L);
      stage_w2(0, 6, 32);
      if (L == 0) w2_regs(ic<4>{}, ic<2>{});
      BARRIER();
      gather_stats(2 * L);
      w1_put(ic<3>{}, ic<1>{});
      if (L == 0 && m < 14) w1_colsum(19, 32);
      HBAR();
      if (L == 0 && m < 14) { __builtin_amdgcn_s_setprio(3); w1_colsum(11, 15); cs_write(cs, L_CS1 + hw * 128); __builtin_amdgcn_s_setprio(0); }
      HBAR();
      v4u st2[20];
      if (L == 0) { fill_x(1); const u32 so = WS_OF + 2 * MB + (T13 * 32 + 2 * m) * 1024; bst16<0>(empt, l16, rws, so); bst16<1024>(empt, l16, rws, so); w2_regs(ic<6>{}, ic<9>{}); WAIT_VM(18); }
      else sfor<10>([&](auto i) { w2_get(&st2[2 * i], 18 + 2 * i); });
      BARRIER();
      const u32 ta = fresh(l16 + (hw >> 1) * 2048);
      if (L == 0) hint(8);
      w2_put(ic<0>{}, ic<6>{});
      HBAR();
      if constexpr (L == 0) { hint(9); ask(8); if (hw != 2) stage_w2(18, 26, -6); hinted(8); }
      else { WAIT_VM(0); sfor<3>([&](auto i) { w2_set(&st2[2 * i], 18 + 2 * i, ta); }); }
      BARRIER();
      if constexpr (L == 0) { hint(10); if (hw == 2) __builtin_amdgcn_s_sleep(6); ask(9); }
      HBAR();
      if constexpr (L == 0) { hint(11); hinted(9); }
      HBAR();
      if constexpr (L == 0) { if (hw == 2) stage_w2(18, 26, -6); stage_w2(26, 38, -6); ask(10); WAIT_VM(0); }
      else sfor<7>([&](auto i) { w2_set(&st2[6 + 2 * i], 24 + 2 * i, ta); });
      HBAR();
      if (L == 0) hinted(10);
      HBAR();
      if (L == 0) { ask(11); fill_h(ic<0>{}, ic<1>{}); }
      w2_put(ic<6>{}, ic<6>{});
      if (L == 0) hinted(11);
      HBAR();
      w2_put(ic<12>{}, ic<3>{});
      HBAR();
      if (L == 0) fill_h(ic<1>{}, ic<1>{});
      HBAR();
      const u32 ka = fresh(L_WQ + hw * 24 * KB + (8 * ((lane >> 4) & 1) + 4 * (lane >> 5) + ((lane >> 2) & 3)) * 64 + (lane & 3) * 16);
      if (L == 0 && hw < 2) sfor<12>([&](auto i) { ldsw<v4u>(ka + i * 1024, keep[i]); });
      HBAR();
      if (L == 0) { hint(7); fill_h(ic<2>{}, ic<1>{}); }
      if (L == 0 && hw < 2) sfor<12>([&](auto i) { ldsw<v4u>(ka + (12 + i) * 1024, keep[12 + i]); });
      BARRIER();
      if constexpr (L == 0) {
        ask(7);
        if (hw >= 2) sfor<24>([&](auto i) { ldsw<v4u>(ka + i * 1024, keep[i]); });
        hinted(7);
        BARRIER();
        gather_stats(1);
      }
    });
    STAMP();
    return;
  }
  v4u res[2], b2p[2];
  v4u qf[2];
  struct Aux { v4u tf; u32 c[4]; float rs; f2 st; f4 rv[4]; };
  auto aux_read = [&](Aux& x, bool ffn) {
    IDS();
    const u32 lane = fresh(lane0), h = lane >> 5, t32 = lane & 31;
    x.st = ldsr<f2>(L_SD + hw * 256 + t32 * 8);
    x.rs = ldsr<float>(L_RS + hw * 128 + t32 * 4);
    if (ffn) { const u32 lc = fresh(L_CS1 + h * 512 + t32 * 4); sfor<4>([&](auto t) { x.c[t] = ldsr<u32>(lc + t * 128); }); }
    else { const u32 lc = fresh(L_CSQ + t32 * 4), lr = fresh(L_RS + hw * 128 + 16 * h);
           sfor<3>([&](auto t) { x.c[t] = ldsr<u32>(lc + t * 128); });
           sfor<4>([&](auto g) { x.rv[g] = ldsr<f4>(lr + 32 * g); }); }
  };
  auto aux_make = [&](Aux& x, bool ffn) { const u32 h = fresh(lane0) >> 5; x.tf = aux_tok(x.st, h); if (!ffn && h) x.tf = (v4u){}; pin(x.tf); };
  v4u kv[4]; u32 lk;
  auto kv_epilogue = [&](u32 so, const Aux& x, f16v& ak, f16v& av) {
    const u32 l16 = fresh(lane0) * 16;
    ak *= x.rs;
    kv[0] = pk8(ak, 0); kv[1] = pk8(ak, 8); bst16<0>(kv[0], l16, rws, so); bst16<1024>(kv[1], l16, rws, so);
    SBAR();
    sfor<16>([&](auto r) { av[(int)r] *= x.rv[r / 4][r % 4]; });
    kv[2] = pk8(av, 0); kv[3] = pk8(av, 8); bst16<2048>(kv[2], l16, rws, so); bst16<3072>(kv[3], l16, rws, so);
    lk = fresh(L_KV + (8 + w0) * 4096 + l16);
  };
  auto q_epilogue = [&](const Aux& x, f16v aq) { aq *= x.rs * (0.25f * 1.4426950408889634f); qf[0] = pk8(aq, 0); qf[1] = pk8(aq, 8); };
  u32 kvf; { IDS(); kvf = (lane0 == 1 || lane0 == 2) && grp + lane0 < 16 ? WS_FLAG + cu * 128 + lane0 * (16 * 128 - 32) + 64 + hw * 8 : 0x7fffffffu; }
  auto kv_flag = [&](int L) { WAIT_VM(0); bst8(tagv, kvf, rws, L * 256 * 128); };
  {
    IDS();
    const u32 lane = lane0, h = lane >> 5, t32 = lane & 31;
    const v4i rx = mk_rsrc(p.x, 2 * MB);
    const u32 vo = wrow * 1024 + (((lane & 3) ^ ((lane >> 4) & 3)) * 16), so = T13 * 32 * 1024, xs = hw * 12 * KB;
    auto issue = [&](int u) { const u32 o = u < 16 ? so + ((u + 2 * rot) & 15) * 64 : T24 * 32 * 1024 + cb * 128 + kh * 64;
                              dma16(xs + (u % 6) * 2048, vo, rx, o); dma16(xs + (u % 6) * 2048 + 1024, vo, rx, o + 16384); };
    sfor<6>([&](auto u) { issue(u); });
    const u32 xa0 = xs + t32 * 64 + ((h ^ ((t32 >> 2) & 3)) * 16), xa1 = xa0 ^ 32;
    v4u xq[32]; v2u bb[4];
    float s1 = 0.f, s2 = 0.f;
    sfor<17>([&](auto u) {
      if constexpr (u < 16) {
        asm volatile("s_waitcnt vmcnt(%0)" ::"i"(2 * ((u == 0 ? 5 : u + 4 < 16 ? u + 4 : 16) - u) + (u > 12 ? 5 : 0)) : "memory");
        xq[2 * u] = ldsr<v4u>(xa0 + (u % 6) * 2048); xq[2 * u + 1] = ldsr<v4u>(xa1 + (u % 6) * 2048);
        SBAR();
      }
      if constexpr (u > 0) {
        pin(xq[2 * u - 2]); pin(xq[2 * u - 1]);
        if constexpr (u + 5 < 17) issue(u + 5);
        if constexpr (u == 12) { load_tags(); const v4i rb = mk_rsrc(p.b2, 1024); const u32 bo = (cb * 64 + kh * 32 + 4 * h) * 2; sfor<4>([&](auto i) { bld8<i * 16, false>(bb[i], bo, rb, 0); }); }
        sfor<8>([&](auto i) { const u32 eu = xq[2 * u - 2 + i / 4][i % 4]; const bf2 e = __builtin_bit_cast(bf2, eu);
                              s1 = __builtin_amdgcn_fdot2_f32_bf16(e, __builtin_bit_cast(bf2, 0x3f803f80u), s1, false); s2 = __builtin_amdgcn_fdot2_f32_bf16(e, e, s2, false); });
        pin(s1); pin(s2);
      }
      SBAR();
    });
    { const f2 st = ln_stats(hsum(s1), hsum(s2)); ldsw<f2>(L_SD + hw * 256 + t32 * 8, st); ldsw<float>(L_RS + hw * 128 + t32 * 4, __builtin_amdgcn_rcpf(st.y)); }
    WAIT_VM(0);
    fix_tags();
    sfor<4>([&](auto i) { pin(bb[i]); });
    sfor<2>([&](auto f) { const u32 ra = xs + (16 % 6) * 2048 + t32 * 64 + 8 * h, z = ((t32 >> 2) & 3) * 16;
                          const v2u a = ldsr<v2u>(ra + ((2 * f * 16) ^ z)), b = ldsr<v2u>(ra + (((2 * f + 1) * 16) ^ z));
                          res[f] = (v4u){a.x, a.y, b.x, b.y}; b2p[f] = (v4u){bb[2 * f].x, bb[2 * f].y, bb[2 * f + 1].x, bb[2 * f + 1].y}; pin(res[f]); });
    const u32 wb0 = fresh(L_WQ + trbase(lane)), wb1 = fresh(wb0 + 48 * KB);
    auto addr = [&](auto i) { constexpr int o = (i / 2) * 3072 + 1024 + (i & 1) * 1024; return o < 48 * KB ? wb0 + o : wb1 + (o - 48 * KB); };
    auto addrq = [&](auto i) { constexpr int o = i * 3072; return o < 48 * KB ? wb0 + o : wb1 + (o - 48 * KB); };
    f16v acc1[2] = {};
    if (STAMPL) STAMP();
    flg_wait(false, 1);
    gemm<2, 32, 0, 32, true, false>(acc1, xq, 0, addr, [&](auto i) { constexpr int n = i + DEP; if constexpr (n == 16 || n == 32 || n == 48 || n == 56) { pin(acc1[0]); pin(acc1[1]); flg_wait(false, n / 8 + (n < 48)); } } RETRY_PASS);
    pin(acc1[0]); pin(acc1[1]);
    flg_wait(false, 8);
    if (STAMPL) STAMP();
    Aux ax; aux_read(ax, false);
    SBAR();
    aux_make(ax, false);
    acc1[0] = mfma(aux_col(ax.c[1]), ax.tf, acc1[0]); acc1[1] = mfma(ax.tf, aux_col(ax.c[2]), acc1[1]);
    kv_epilogue(WS_KV + (T13 * 16 + m) * 4096, ax, acc1[0], acc1[1]);
    flg_wait(true, 7);
    f16v accq = {};
    gemm<1, 32, 0, 32, false, false>(&accq, xq, 0, addrq, [&](auto i) {
      if constexpr (i == 14) { kv_flag(0); LBAR(); ax.c[0] = ldsr<u32>(L_CSQ + t32 * 4); }
      if constexpr (i % 4 == 0 && i >= 16) ldsw<v4u>(lk + (i / 4 - 4) * 1024, kv[i / 4 - 4]);
      if constexpr (i == 22) accq = mfma(aux_col(ax.c[0]), ax.tf, accq); } RETRY_PASS);
    q_epilogue(ax, accq);
  }
  for (int L = 0;; ++L) {
    IDS();
    const u32 lane = fresh(lane0), l16 = lane * 16, h = lane >> 5, t32 = lane & 31;
    auto res_init = [&](f16v (&acc)[2], bool bias) {
      sfor<8>([&](auto i) { const u32 r = res[i / 4][i % 4], b = bias ? fresh(b2p[i / 4][i % 4]) : 0u;
                            acc[0][2 * i] = bias ? blo(r) + blo(b) : blo(r); acc[0][2 * i + 1] = bias ? bhi(r) + bhi(b) : bhi(r); });
      acc[1] = (f16v){};
      pin(acc[0]);
    };
    auto res_epilogue = [&](f16v (&acc)[2], int buf, bool last) {
      const u32 xa = fresh(L_XCH + w * 4096 + l16), xb = fresh(L_XCH + (w ^ 2) * 4096 + l16);
      const u32 so = sfresh(WS_XF + buf * 2 * MB + (T24 * 32 + cb * 4 + kh * 2) * 1024), ss = sfresh(WS_ST + buf * 256 * KB + (T24 * 16 + cb * 2 + kh) * 256);
      sfor<4>([&](auto g) { ldsw<f4>(xa + g * 1024, (f4){acc[1][4 * g], acc[1][4 * g + 1], acc[1][4 * g + 2], acc[1][4 * g + 3]}); });
      BARRIER();
      f4 o[4]; sfor<4>([&](auto g) { o[g] = ldsr<f4>(xb + g * 1024); });
      f16v v; sfor<16>([&](auto r) { v[(int)r] = acc[0][(int)r] + o[r / 4][r % 4]; });
      res[0] = canon(pk8(v, 0)); res[1] = canon(pk8(v, 8));
      if (!last) {
        bst16<0>(res[0], l16, rws, so); bst16<1024>(res[1], l16, rws, so);
        SBAR();
        float s1 = 0.f, s2 = 0.f;
        sfor<8>([&](auto i) { const u32 eu = res[i / 4][i % 4]; const bf2 e = __builtin_bit_cast(bf2, eu);
                              s1 = __builtin_amdgcn_fdot2_f32_bf16(e, __builtin_bit_cast(bf2, 0x3f803f80u), s1, false); s2 = __builtin_amdgcn_fdot2_f32_bf16(e, e, s2, false); });
        s1 = hsum(s1); s2 = hsum(s2);
        bst8((v2u){canon(fb(s1)), canon(fb(s2))}, t32 * 8, rws, ss);
        WAIT_VM(0);
        BARRIER();
      } else {
        GAS unsigned char* op = (GAS unsigned char*)p.out + (T24 * 32 + t32) * 1024 + (cb * 64 + kh * 32 + 8 * h) * 2;
        sfor<2>([&](auto f) {
          auto a = __builtin_amdgcn_permlane32_swap(res[f].x, res[f].z, false, false), b = __builtin_amdgcn_permlane32_swap(res[f].y, res[f].w, false, false);
          *(GAS v4u*)(op + f * 32) = (v4u){a[0], b[0], a[1], b[1]}; });
      }
    };
    {
      Attn at; at.init(lane, hw); at.q[0] = qf[0]; at.q[1] = qf[1];
      at.diag();
      BARRIER();
      at.rest([&]() { LBAR();   });
      const u32 so = WS_OF + L * 2 * MB + (T13 * 32 + 2 * m) * 1024; const int jmin = max(8 - T13, 0);
      bst16<0>(at.finish<0>(jmin), l16, rws, so); bst16<1024>(at.finish<1>(jmin), l16, rws, so);
    }
    BARRIER();
    WAIT_VM(0);
    BARRIER();
    {
      constexpr int LOOK = 10;
      v4u win[LOOK];
      f16v acc2[2];
      const u32 so = WS_OF + L * 2 * MB + (T24 * 32 + kh * 16) * 1024, wb = fresh(L_WO + kh * 33 * KB + trbase(lane)), wc = fresh(wb ^ 1024);
      auto issue = [&](auto i, v4u& d) { bld16<(i & 3) * 1024, true>(d, l16, rws, so + (((i >> 2) + rot2) & 3) * 4096); };
      auto addr = [&](auto i) { return ((i & 1) ? wc : wb) + (i / 2) * 2048; };
      sfor<LOOK>([&](auto i) { issue(i, win[i]); });
      res_init(acc2, false);
      gemm<2, 16, LOOK, LOOK, false, true>(acc2, win, issue, addr, [&](auto i) { if constexpr (i == 16) LBAR();   } RETRY_PASS);
      res_epilogue(acc2, 2 * L, false);
    }
    const u32 soh = WS_HF + L * 7 * MB + (T24 * 112 + kh * 56) * 1024;
    constexpr int LOOK4 = 12;
    v4u hw4[LOOK4];
    u32 pb[7];
    auto h_bases = [&]() { sfor<7>([&](auto j) { const int jj = j + rot4; pb[j] = soh + (jj >= 7 ? jj - 7 : jj) * 8192; }); };
    if (m < 14) {
      constexpr int LOOK = 22;
      v4u win[LOOK];
      f16v acc3[4] = {};
      const u32 so = WS_XF + 2 * L * 2 * MB + T13 * 32 * 1024, wb0 = fresh(L_W1 + trbase(lane)), wb1 = fresh(wb0 + 63 * KB), wb2 = fresh(wb0 + 126 * KB);
      const u32 sh = WS_HF + L * 7 * MB + (T13 * 112 + m * 8) * 1024;
      auto issue = [&](auto i, v4u& d) { bld16<(i & 3) * 1024, true>(d, l16, rws, so + (((i >> 2) + rot) & 7) * 4096); };
      auto addr = [&](auto i) { constexpr int o = ((i + 60) & 127) * 1024; return o < 63 * KB ? wb0 + o : o < 126 * KB ? wb1 + (o - 63 * KB) : wb2 + (o - 126 * KB); };
      sfor<8>([&](auto i) { issue(i, win[i]); });
      Aux ax;
      constexpr float K1 = -2.0f * 0.7978845608028654f * 1.4426950408889634f, K2 = K1 * 0.044715f;
      float c1, c2, za, zb; v4u q;
      auto gelu_stage = [&](auto e, auto g) {
        const float ua = acc3[e / 8][2 * (e % 8)], ub = acc3[e / 8][2 * (e % 8) + 1], sd = ax.st.y;
        if constexpr (g == 0) { za = __builtin_fmaf(ua * ua, c2, c1); zb = __builtin_fmaf(ub * ub, c2, c1); }
        if constexpr (g == 1) { za = ex2(za * ua); zb = ex2(zb * ub); }
        if constexpr (g == 2) { za = __builtin_amdgcn_rcpf(__builtin_fmaf(za, sd, sd)); zb = __builtin_amdgcn_rcpf(__builtin_fmaf(zb, sd, sd)); }
        if constexpr (g == 3) { za *= ua; zb *= ub; }
        pin(za); pin(zb);
        if constexpr (g == 3) { u32 r = canon(pk(za, zb)); pin(r); q[e % 4] = r; if constexpr (e % 4 == 3) bst16<((e / 4) & 3) * 1024>(q, l16, rws, sh); }
      };
      gemm<4, 32, LOOK, LOOK, false, true, 8, 16>(acc3, win, issue, addr, [&](auto i) {
        if constexpr (i == 40) LBAR();
        if constexpr (i == 60) { LBAR(); aux_read(ax, true); }
        if constexpr (i == 66) { aux_make(ax, true); c1 = K1 * ax.rs; c2 = K2 * ax.rs * ax.rs * ax.rs; pin(c1); pin(c2); }
        if constexpr (i >= 70 && i < 74) acc3[i & 3] = mfma(aux_col(ax.c[i & 3]), ax.tf, acc3[i & 3]);
        if constexpr (i >= 80) gelu_stage(ic<(i - 80) / 4>{}, ic<i % 4>{});
      } RETRY_PASS);
      SBAR(); if (L == 0) WAIT_VM(1);
      BARRIER();
      sfor<5>([&](auto f_) {
        constexpr int f = 3 + f_;
        sfor<4>([&](auto d) {
          const f2 u = {acc3[f / 2][(f & 1) * 8 + 2 * d], acc3[f / 2][(f & 1) * 8 + 2 * d + 1]}, sd = {ax.st.y, ax.st.y};
          const f2 z = u * __builtin_elementwise_fma(u * u, (f2){c2, c2}, (f2){c1, c1});
          const f2 dn = __builtin_elementwise_fma((f2){ex2(z.x), ex2(z.y)}, sd, sd);
          const f2 g = u * (f2){__builtin_amdgcn_rcpf(dn.x), __builtin_amdgcn_rcpf(dn.y)};
          q[(int)d] = canon(pk(g.x, g.y));
        });
        bst16<(f & 3) * 1024>(q, l16, rws, sh + (f >> 2) * 4096);
        SBAR();
        if constexpr (f == 4) {
          h_bases(); const u32 vo = L == 1 ? l16 : 0x7fffffffu;
          sfor<LOOK4>([&](auto i) { constexpr int k = w2_kstep(i); bld16<(k & 3) * 1024, true>(hw4[i], vo, rws, pb[k >> 3]); });
          SBAR();
        }
        if constexpr (f == 5) { if (L == 0) WAIT_VM(14); LBAR(); }
      });
    } else {
      LBAR(); LBAR(); BARRIER(); LBAR();
      h_bases();
      sfor<LOOK4>([&](auto i) { hw4[i] = (v4u){}; });
#pragma unroll 1
      for (u32 spins = 0; spins < SPIN_CAP && L == 1; ++spins) {
        __builtin_amdgcn_s_sleep(8);
        bld16<2048, true>(hw4[0], l16, rws, soh); bld16<3072, true>(hw4[1], l16, rws, soh); wait_vm<0>(hw4[0], hw4[1]);
        if (!empty2(hw4[0], hw4[1])) break;
      }
    }
    {
      f16v acc2[2];
      const u32 wb0 = fresh(kh * 3072 + trbase(lane)), wb1 = fresh(wb0 + 60 * KB), wb2 = fresh(wb0 + 120 * KB), wc0 = fresh(wb0 ^ 1024), wc1 = fresh(wb1 ^ 1024), wc2 = fresh(wb2 ^ 1024);
      const u32 l16b = l16 + 4096;
      auto issue = [&](auto i, v4u& d) { constexpr int k = w2_kstep(i); bld16<(k & 3) * 1024, true>(d, (k & 4) ? l16b : l16, rws, pb[k >> 3]); };
      auto addr = [&](auto i) { constexpr int o = w2_pos(i / 2) * 4096; constexpr bool c = i & 1;
                                return o < 60 * KB ? (c ? wc0 : wb0) + o : o < 120 * KB ? (c ? wc1 : wb1) + (o - 60 * KB) : (c ? wc2 : wb2) + (o - 120 * KB); };
      res_init(acc2, true);
      if (L == 0) WAIT_VM(2);
      BARRIER();
      if (L == 0 || m >= 14) sfor<LOOK4>([&](auto i) { constexpr int k = w2_kstep(i); bld16_rw<(k & 3) * 1024>(hw4[i], l16, rws, pb[k >> 3]); });
      gemm<2, 56, LOOK4, LOOK4, false, true>(acc2, hw4, issue, addr, [&](auto i) { if constexpr (i == 0 || i == 8 || i == 36 - DEP || i == 33 || i == 63 || i == 76 - DEP || i == 75 || i == 96) LBAR(); } RETRY_PASS);
      res_epilogue(acc2, 1, L == 1);
    }
    if (L == 1) break;
    {
      constexpr int LOOK = 12;
      const u32 so = WS_XF + 1 * 2 * MB + T13 * 32 * 1024, wb0 = fresh(L_WQ + trbase(lane)), wb1 = fresh(wb0 + 48 * KB);
      auto issue = [&](auto i, v4u& d) { bld16<(i & 3) * 1024, true>(d, l16, rws, so + (((i >> 2) + rot) & 7) * 4096); };
      auto addr = [&](auto i) { constexpr int o = (i / 2) * 3072 + 1024 + (i & 1) * 1024; return o < 48 * KB ? wb0 + o : wb1 + (o - 48 * KB); };
      auto addrq = [&](auto i) { constexpr int o = i * 3072; return o < 48 * KB ? wb0 + o : wb1 + (o - 48 * KB); };
      const u32 sk = sfresh(WS_KV + 4 * MB + (T13 * 16 + m) * 4096);
      f16v acc1[2] = {};
      v4u xq[32];
      sfor<LOOK>([&](auto i) { issue(i, xq[i]); });
      Aux ax;
      gemm<2, 32, LOOK, 32, true, true>(acc1, xq, issue, addr, [&](auto i) {
        if constexpr (i == 40) { LBAR(); aux_read(ax, false); }
        if constexpr (i == 48) aux_make(ax, false);
        if constexpr (i == 52) acc1[0] = mfma(aux_col(ax.c[1]), ax.tf, acc1[0]);
        if constexpr (i == 53) acc1[1] = mfma(ax.tf, aux_col(ax.c[2]), acc1[1]);
      } RETRY_PASS);
      kv_epilogue(sk, ax, acc1[0], acc1[1]);
      f16v accq = mfma(aux_col(ax.c[0]), ax.tf, (f16v){});
      gemm<1, 32, 0, 32, false, false>(&accq, xq, 0, addrq, [&](auto i) {
        if constexpr (i % 4 == 1 && i < 16) ldsw<v4u>(lk + (i / 4) * 1024, kv[i / 4]);
        if constexpr (i == 17) { kv_flag(1); LBAR();   } } RETRY_PASS);
      q_epilogue(ax, accq);
    }
  }
  STAMP(); STAMPCLK(1);
  { IDS(); if (w == 0 && m == 0 && lane0 == 0) bst8(tagv, 0, rws, WS_EPOCH + grp * 256); }
}

extern "C" void kernel_launch(void* const* d_in, const int* in_sizes, int n_in, void* d_out, int out_size, void* d_ws, size_t ws_size, hipStream_t stream) {
  Params p;
  p.x = d_in[0]; p.wqkv = d_in[1]; p.wo = d_in[2]; p.w1 = d_in[4]; p.b1 = d_in[5]; p.w2 = d_in[6]; p.b2 = d_in[7]; p.out = d_out; p.ws = (unsigned char*)d_ws;
  (void)hipFuncSetAttribute((const void*)fwd, hipFuncAttributeMaxDynamicSharedMemorySize, LDS_BYTES);
  hipLaunchKernelGGL(fwd, dim3(256), dim3(512), LDS_BYTES, stream, p);
}
```

```cpp
#include <hip/hip_runtime.h>
#include <type_traits>
#include <utility>

#define CODE_KB 72
#define CPRIO 0
#define OUTSC 0
#define KVI 17
#define LK4 12
#define DEPV 5
#define LPRIO 0
#define KVDELAY 40
#define QSTART 2
#define MAP 1
#define STAMPS 0

#define GAS __attribute__((address_space(1)))
#define LAS __attribute__((address_space(3)))
#define DI __device__ __forceinline__
typedef unsigned u32;
typedef unsigned long long u64;
typedef u32 v4u __attribute__((ext_vector_type(4)));
typedef u32 v2u __attribute__((ext_vector_type(2)));
typedef int v4i __attribute__((ext_vector_type(4)));
typedef short s4 __attribute__((ext_vector_type(4)));
typedef __bf16 bf8 __attribute__((ext_vector_type(8)));
typedef __bf16 bf2 __attribute__((ext_vector_type(2)));
typedef float f2 __attribute__((ext_vector_type(2)));
typedef float f4 __attribute__((ext_vector_type(4)));
typedef float f16v __attribute__((ext_vector_type(16)));
template <int I> using ic = std::integral_constant<int, I>;

template <class F, int... I> DI void sfor_impl(F&& f, std::integer_sequence<int, I...>) { (f(ic<I>{}), ...); }
template <int N, class F> DI void sfor(F&& f) { sfor_impl(f, std::make_integer_sequence<int, N>{}); }

constexpr int KB = 1024, MB = 1024 * 1024;
constexpr int WS_EPOCH = 0;
constexpr int WS_FLAG = 32 * KB;
constexpr int WS_XF = 1 * MB;
constexpr int WS_ST = WS_XF + 6 * MB;
constexpr int WS_KV = WS_ST + 1 * MB;
constexpr int WS_OF = WS_KV + 8 * MB;
constexpr int WS_HF = WS_OF + 4 * MB;
constexpr int WS_STAMP = WS_HF + 14 * MB;
constexpr int WS_END = WS_STAMP + (STAMPS ? MB : 0);
constexpr int L_KV = 0, L_ONES = 48 * KB, L_TA = 54 * KB, L_TB = 58 * KB, L_WQ = 60 * KB, L_WO = 92 * KB, L_W1 = 0, L_XCH = 44 * KB;
constexpr int L_SD = 156 * KB, L_RS = L_SD + 1024, L_CSQ = L_RS + 512, L_CS1 = L_CSQ + 384, L_B1 = L_CS1 + 512, L_ZERO = L_B1 + 512, L_FLG = L_ZERO + 16, LDS_BYTES = 160 * KB;

struct Params { const void *x, *wqkv, *wo, *w1, *b1, *w2, *b2; void* out; unsigned char* ws; };

DI v4i mk_rsrc(const void* p, u32 bytes) { v4i r; r.x = (int)(u32)(u64)p; r.y = (int)((u32)((u64)p >> 32) & 0xffffu); r.z = (int)bytes; r.w = 0x00020000; return r; }
template <int OFF, bool SC1> DI void bld16(v4u& d, u32 voff, v4i rs, u32 soff) {
  if constexpr (SC1) asm volatile("buffer_load_dwordx4 %0, %1, %2, %3 offen offset:%4 sc1" : "=v"(d) : "v"(voff), "s"(rs), "s"(soff), "i"(OFF));
  else asm volatile("buffer_load_dwordx4 %0, %1, %2, %3 offen offset:%4" : "=v"(d) : "v"(voff), "s"(rs), "s"(soff), "i"(OFF));
}
template <int OFF> DI void bld16_rw(v4u& d, u32 voff, v4i rs, u32 soff) { asm volatile("buffer_load_dwordx4 %0, %1, %2, %3 offen offset:%4 sc1" : "+v"(d) : "v"(voff), "s"(rs), "s"(soff), "i"(OFF)); }
template <int OFF, bool SC1> DI void bld8(v2u& d, u32 voff, v4i rs, u32 soff) {
  if constexpr (SC1) asm volatile("buffer_load_dwordx2 %0, %1, %2, %3 offen offset:%4 sc1" : "=v"(d) : "v"(voff), "s"(rs), "s"(soff), "i"(OFF));
  else asm volatile("buffer_load_dwordx2 %0, %1, %2, %3 offen offset:%4" : "=v"(d) : "v"(voff), "s"(rs), "s"(soff), "i"(OFF));
}
template <int OFF> DI void bst16(v4u d, u32 voff, v4i rs, u32 soff) {
  asm volatile("buffer_store_dwordx4 %0, %1, %2, %3 offen offset:%4 sc1\n\ts_nop 1" ::"v"(d), "v"(voff), "s"(rs), "s"(soff), "i"(OFF));
}
DI void bst8(v2u d, u32 voff, v4i rs, u32 soff) { asm volatile("buffer_store_dwordx2 %0, %1, %2, %3 offen sc1" ::"v"(d), "v"(voff), "s"(rs), "s"(soff)); }
DI void touch(u32& d, u32 voff, v4i rs, u32 soff) { asm volatile("buffer_load_dword %0, %1, %2, %3 offen" : "=v"(d) : "v"(voff), "s"(rs), "s"(soff)); }
template <bool SC1 = false> DI void dma16(u32 lds, u32 voff, v4i rs, u32 soff) {
  if constexpr (SC1) asm volatile("s_mov_b32 m0, %0\n\ts_nop 0\n\tbuffer_load_dwordx4 %1, %2, %3 offen sc1 lds" ::"s"(lds), "v"(voff), "s"(rs), "s"(soff));
  else asm volatile("s_mov_b32 m0, %0\n\ts_nop 0\n\tbuffer_load_dwordx4 %1, %2, %3 offen lds" ::"s"(lds), "v"(voff), "s"(rs), "s"(soff));
}
template <int N, class T> DI void wait_vm(T& x) { asm volatile("s_waitcnt vmcnt(%1)" : "+v"(x) : "i"(N)); }
template <int N, class T> DI void wait_vm(T& x, T& y) { asm volatile("s_waitcnt vmcnt(%2)" : "+v"(x), "+v"(y) : "i"(N)); }
template <class T> DI void pin(T& x) { asm volatile("" : "+v"(x)); }
#define WAIT_VM(n) asm volatile("s_waitcnt vmcnt(" #n ")" ::: "memory")
#define BARRIER() do { SBAR(); if (STAMPS != 3) STAMP(); asm volatile("s_waitcnt lgkmcnt(0)\n\ts_barrier" ::: "memory"); SBAR(); } while (0)
#define STAMPL 0
#define LBAR() do { SBAR(); if (STAMPL) STAMP(); asm volatile("s_barrier" ::: "memory"); SBAR(); } while (0)
#define HBAR() do { if (STAMPL) STAMP(); asm volatile("s_waitcnt lgkmcnt(0)\n\ts_barrier" ::: "memory"); } while (0)
#define SBAR() __builtin_amdgcn_sched_barrier(0)
DI f16v mfma(v4u a, v4u b, f16v c) { return __builtin_amdgcn_mfma_f32_32x32x16_bf16(__builtin_bit_cast(bf8, a), __builtin_bit_cast(bf8, b), c, 0, 0, 0); }
DI u32 pk(float lo, float hi) { bf2 r = __builtin_convertvector((f2){lo, hi}, bf2); return __builtin_bit_cast(u32, r); }
DI float blo(u32 v) { return __builtin_bit_cast(float, v << 16); }
DI float bhi(u32 v) { return __builtin_bit_cast(float, v & 0xffff0000u); }
template <class T> DI T ldsr(u32 a) { return *(LAS T*)(size_t)a; }
template <class T> DI void ldsw(u32 a, T v) { *(LAS T*)(size_t)a = v; }
DI v4u trfrag(u32 a) {
  const v2u lo = __builtin_bit_cast(v2u, __builtin_amdgcn_ds_read_tr16_b64_v4i16((LAS s4*)(size_t)a));
  const v2u hi = __builtin_bit_cast(v2u, __builtin_amdgcn_ds_read_tr16_b64_v4i16((LAS s4*)(size_t)(a + 512)));
  return (v4u){lo.x, lo.y, hi.x, hi.y};
}
DI f2 halves(float v) {
  auto r = __builtin_amdgcn_permlane32_swap(__builtin_bit_cast(u32, v), __builtin_bit_cast(u32, v), false, false);
  const u32 a = r[0], b = r[1]; return (f2){__builtin_bit_cast(float, a), __builtin_bit_cast(float, b)};
}
DI float bf(u32 v) { return __builtin_bit_cast(float, v); }
DI u32 fb(float v) { return __builtin_bit_cast(u32, v); }
DI float fadd(float a, float b) { float r; asm("v_add_f32 %0, %1, %2" : "=v"(r) : "v"(a), "v"(b)); return r; }
DI float hsum(float v) { const f2 r = halves(v); return r.x + r.y; }
DI float hmax(float v) { const f2 r = halves(v); return __builtin_fmaxf(r.x, r.y); }
DI float mx3(float a, float b, float c) { return __builtin_fmaxf(__builtin_fmaxf(a, b), c); }
DI float max16(const f16v& s, float m) {
  float a = mx3(m, s[0], s[1]), b = mx3(s[2], s[3], s[4]);
  a = mx3(a, s[5], s[6]); b = mx3(b, s[7], s[8]); a = mx3(a, s[9], s[10]); b = mx3(b, s[11], s[12]); a = mx3(a, s[13], s[14]);
  return mx3(a, b, s[15]);
}
DI v4u pk8(const f16v& v, int o) { return (v4u){pk(v[o], v[o + 1]), pk(v[o + 2], v[o + 3]), pk(v[o + 4], v[o + 5]), pk(v[o + 6], v[o + 7])}; }
#define SPIN_LOG 12
constexpr u32 SPIN_CAP = 1u << SPIN_LOG;
DI u32 fresh(u32 v) { asm volatile("" : "+v"(v)); return v; }
DI u32 sfresh(u32 v) { asm volatile("" : "+s"(v)); return v; }
constexpr u32 EMPTY = 0xffffffffu;
DI u32 umax3(u32 a, u32 b, u32 c) { return max(max(a, b), c); }
DI u32 canon(u32 v) { return min(v, EMPTY - 1); }
DI v4u canon(v4u v) { return (v4u){canon(v.x), canon(v.y), canon(v.z), canon(v.w)}; }
DI bool empty2(const v4u& a, const v4u& b) { return __any(max(umax3(a.x, a.y, a.z), umax3(umax3(a.w, b.x, b.y), b.z, b.w)) == EMPTY); }

#define RETRY_ARG
#define RETRY_PASS
#define RETRY_INC()
template <int P, int N, int NX> DI u32 validate1(v4u (&x)[NX]) {
  v4u &a = x[P % NX], &b = x[(P + 1) % NX];
  wait_vm<N>(a, b);
  u32 v = umax3(umax3(a.x, a.y, a.z), a.w, b.x); pin(v); return v;
}
template <int P, int NX> DI u64 validate2(u32 v, v4u (&x)[NX]) { const v4u& b = x[(P + 1) % NX]; u64 e = __ballot(max(umax3(v, b.y, b.z), b.w) == EMPTY); asm volatile("" : "+s"(e)); return e; }
template <int P, int NX, class FI> DI void validate3(u64 e, v4u (&x)[NX], FI&& issue RETRY_ARG) {
  v4u &a = x[P % NX], &b = x[(P + 1) % NX];
  if (__builtin_expect(e != 0, 0)) {
#pragma unroll 1
    for (u32 spins = 0; spins < SPIN_CAP; ++spins) {
      __builtin_amdgcn_s_sleep(4); RETRY_INC();
      issue(ic<P>{}, a); issue(ic<P + 1>{}, b);
      wait_vm<0>(a, b);
      if (!empty2(a, b)) break;
    }
  }
}
template <int N> DI void wait_lgkm() { __builtin_amdgcn_s_waitcnt(0xc07f | (N << 8)); }
constexpr int cmin(int a, int b) { return a < b ? a : b; }
constexpr int issued(int s, int KS, int LOOK, int INIT) { return s == 0 ? INIT : cmin(cmin(INIT + 2 * s, ((s - 1) & ~1) + LOOK), KS); }
constexpr int DEP = DEPV;
template <int NT, int KS, int KM> constexpr int ord_s(int i) { return i < KM * NT ? i / NT : KM + (i - KM * NT) % (KS > KM ? KS - KM : 1); }
template <int NT, int KS, int KM> constexpr int ord_t(int i) { return i < KM * NT ? i % NT : (i - KM * NT) / (KS > KM ? KS - KM : 1); }
template <int NT, int KS, int LOOK, int NX, bool SWAPLAST, bool VAL, int INIT = LOOK, int KM = KS, class FI, class FA, class FH>
DI void gemm(f16v* acc, v4u (&x)[NX], FI&& issue, FA&& addr, FH&& hook RETRY_ARG) {
  constexpr int N1 = KS * NT;
  v4u ring[DEP + 1];
  u32 vv; u64 ve;
  auto unit = [&](auto i) { return addr(ic<ord_s<NT, KS, KM>(i) * NT + ord_t<NT, KS, KM>(i)>{}); };
  sfor<DEP>([&](auto i) { ring[i] = trfrag(unit(i)); });
  SBAR();
  if constexpr (LOOK > 0 && VAL) { vv = validate1<0, INIT - 2>(x); ve = validate2<0>(vv, x); validate3<0>(ve, x, issue RETRY_PASS); }
  if constexpr (LOOK > 0 && !VAL) wait_vm<INIT - 2>(x[0], x[1]);
  sfor<N1>([&](auto i_) {
    constexpr int i = i_, s = ord_s<NT, KS, KM>(i), t = ord_t<NT, KS, KM>(i);
    if constexpr ((i & 1) == 0 && i + DEP <= N1) { wait_lgkm<2 * DEP - 4>(); SBAR(); }
    if constexpr (SWAPLAST && t == NT - 1) acc[t] = mfma(x[s % NX], ring[i % (DEP + 1)], acc[t]);
    else acc[t] = mfma(ring[i % (DEP + 1)], x[s % NX], acc[t]);
    SBAR();
    hook(i_);
    SBAR();
    if constexpr (i + DEP < N1) ring[(i + DEP) % (DEP + 1)] = trfrag(unit(ic<i + DEP>{}));
    if constexpr (LOOK > 0 && i < KM * NT) {
      constexpr int f0 = issued(s, KS, LOOK, INIT), f1 = issued(s + 1, KS, LOOK, INIT);
      static_assert(NT > 2 || (s & 1) == 0 || f1 == f0, "two-tile loops issue in even steps only");
      if constexpr (t < 2 && f0 + t < f1) { SBAR(); issue(ic<f0 + t>{}, x[(f0 + t) % NX]); }
      constexpr int G = KM == KS ? 2 * NT : NT, g = i % G, P = (i / G + 1) * 2;
      static_assert(KM == KS || 2 * KM == KS, "tail = half of the steps");
      if constexpr (P < KS) {
        if constexpr (VAL) {
          if constexpr (g == G - 3) { SBAR(); vv = validate1<P, f1 - (P + 2)>(x); }
          if constexpr (g == G - 2) { SBAR(); ve = validate2<P>(vv, x); }
          if constexpr (g == G - 1) { SBAR(); validate3<P>(ve, x, issue RETRY_PASS); }
        } else if constexpr (g == G - 1) { SBAR(); wait_vm<f1 - (P + 2)>(x[P % NX], x[(P + 1) % NX]); }
      }
    }
    SBAR();
  });
}

#define STAMP() do {} while (0)
#define STAMPCLK(i) do {} while (0)
DI f2 ln_stats(float s1, float s2) {
  const float mean = s1 * (1.0f / 512.0f), var = __builtin_fmaxf(s2 * (1.0f / 512.0f) - mean * mean, 0.0f);
  return (f2){-mean, __builtin_amdgcn_sqrtf(var + 1e-5f)};
}
DI u32 trbase(u32 lane) { return (4 * (lane >> 5) + ((lane & 15) >> 2)) * 64 + ((lane >> 4) & 1) * 32 + (lane & 3) * 8; }
DI float ex2(float x) { return __builtin_amdgcn_exp2f(x); }
DI v4u expk8(const f16v& s, int o) { return (v4u){pk(ex2(s[o]), ex2(s[o + 1])), pk(ex2(s[o + 2]), ex2(s[o + 3])), pk(ex2(s[o + 4]), ex2(s[o + 5])), pk(ex2(s[o + 6]), ex2(s[o + 7]))}; }
DI v4u aux_tok(f2 st, u32 h) {
  const float v = h ? st.y : st.x; const u32 a = pk(v, v) & 0xffffu; const float r = v - blo(a); const u32 b = pk(r, r) & 0xffffu; const float r2 = r - blo(b);
  return (v4u){a | (b << 16), h ? pk(r2, 0.0f) : a, 0u, 0u};
}
DI v4u aux_col(u32 v) { return (v4u){(v & 0xffffu) | (v << 16), v >> 16, 0u, 0u}; }

struct Attn {
  v4u q[2], kf, v0, v1, p0, p1, t0, t1; f16v o[2], nm[2], sc[2]; u32 ka, va, vb, vstr, l16;
  DI void init(u32 lane, int qb) {
    l16 = lane * 16;
    const bool real = (lane & 16) == 0;
    ka = L_KV + qb * 4096 + l16; vstr = real ? 4096u : 512u;
    va = real ? L_KV + qb * 4096 + 2048 + l16 : L_ONES + qb * 512 + (lane & 15) * 16; vb = real ? va + 1024 : va;
  }
  DI f16v addmask() { f16v c; sfor<4>([&](auto g) { const f4 t = ldsr<f4>(L_TA + g * 1024 + l16); sfor<4>([&](auto i) { c[4 * g + i] = t[(int)i]; }); }); return c; }
  template <int D, int MASK, bool QK, bool KN, int PV> DI void stage(u32 a0, u32 a1, u32 kn) {
    const f16v& si = sc[D];
    float e[16];
    sfor<4>([&](auto i) { e[i] = ex2(si[(int)i]); });
    SBAR();
    wait_lgkm<0>();
    if constexpr (QK) sc[1 - D] = mfma(kf, q[1 - D], nm[1 - D]);
    SBAR();
    if constexpr (KN) kf = ldsr<v4u>(kn);
    sfor<4>([&](auto i) { e[4 + i] = ex2(si[4 + i]); });
    SBAR();
    if constexpr (PV == 1) o[1 - D] = mfma(v0, p0, o[1 - D]);
    if constexpr (PV == 2) o[1 - D] = mfma(v0, p0, (f16v){});
    SBAR();
    v0 = ldsr<v4u>(a0);
    sfor<4>([&](auto i) { e[8 + i] = ex2(si[8 + i]); });
    SBAR();
    if constexpr (PV) o[1 - D] = mfma(v1, p1, o[1 - D]);
    SBAR();
    v1 = ldsr<v4u>(a1);
    sfor<4>([&](auto i) { e[12 + i] = ex2(si[12 + i]); });
    SBAR();
    sfor<4>([&](auto i) { p0[(int)i] = pk(e[2 * i], e[2 * i + 1]); });
    sfor<4>([&](auto i) { p1[(int)i] = pk(e[8 + 2 * i], e[9 + 2 * i]); });
    if constexpr (MASK == 1) { pin(t0); pin(t1); p0 &= ~t0; p1 &= ~t1; }
    if constexpr (MASK == 2) { p0 &= t0; p1 &= t1; }
    pin(p0); pin(p1);
    SBAR();
  }
  DI void diag() {
    const f16v c = addmask();
    v4u kd[2];
    sfor<2>([&](auto d) { kd[d] = ldsr<v4u>(ka + 8 * 4096 + d * 1024); });
    t0 = ldsr<v4u>(L_TB + l16); t1 = ldsr<v4u>(L_TB + 1024 + l16);
    SBAR();
    sfor<2>([&](auto d) { sc[d] = mfma(kd[d], q[d], c); });
    va = fresh(va + 8 * vstr); vb = fresh(vb + 8 * vstr);
    SBAR();
    sfor<2>([&](auto d) {
      const float mx = hmax(max16(sc[d], sc[d][0]));
      sfor<16>([&](auto r) { nm[d][(int)r] = -mx; });
      pin(nm[d]);
      sc[d] = mfma(kd[d], q[d], nm[d]);
      SBAR();
    });
    stage<0, 1, false, false, 0>(va, vb, 0);
    stage<1, 1, false, false, 2>(va + 256, vb + 256, 0);
  }
  template <class F> DI void rest(F&& hint) {
    sc[0] = mfma(ldsr<v4u>(ka + 7 * 4096), q[0], nm[0]); kf = ldsr<v4u>(ka + 7 * 4096 + 1024);
    sfor<8>([&](auto i) {
      constexpr int j = 7 - i;
      va = fresh(va - vstr); vb = fresh(vb - vstr);
      if constexpr (j == 0) hint();
      stage<0, j == 0 ? 2 : 0, true, (j > 0), j == 7 ? 2 : 1>(va, vb, ka + (j - 1) * 4096);
      stage<1, j == 0 ? 2 : 0, (j > 0), (j > 0), 1>(va + 256, vb + 256, ka + (j - 1) * 4096 + 1024);
    });
    o[1] = mfma(v0, p0, o[1]); o[1] = mfma(v1, p1, o[1]);
  }
  template <int D> DI v4u finish(int jmin) {
    f16v& oo = o[D];
    float big = __builtin_fabsf(oo[0]); sfor<7>([&](auto r) { big += __builtin_fabsf(oo[r + 1]); });
    if (__builtin_expect(!__all(oo[8] <= 0x1p60f && big <= 0x1p126f), 0)) {
      auto scores = [&](int j) {
        f16v c = addmask();
        if (j != 8) sfor<16>([&](auto r) { c[(int)r] = (j == 0 && c[(int)r] == 0.0f) ? -__builtin_inff() : 0.0f; });
        return mfma(ldsr<v4u>(ka + j * 4096 + D * 1024), q[D], c);
      };
      float mx = -__builtin_inff();
#pragma unroll 1
      for (int j = jmin; j <= 8; ++j) mx = max16(scores(j), mx);
      mx = hmax(mx);
      oo = (f16v){};
#pragma unroll 1
      for (int j = jmin; j <= 8; ++j) {
        const f16v s = scores(j) - mx;
        oo = mfma(ldsr<v4u>(va + j * vstr + D * 256), expk8(s, 0), oo); oo = mfma(ldsr<v4u>(vb + j * vstr + D * 256), expk8(s, 8), oo);
      }
    }
    const float rl = __builtin_amdgcn_rcpf(oo[8]);
    sfor<8>([&](auto r) { oo[(int)r] *= rl; });
    return canon(pk8(oo, 0));
  }
};

constexpr int w2_kstep(int i) { return ((i % 14) / 2) * 8 + 2 * (i / 14) + (i & 1); }
constexpr int w2r_slot(int i) { return i < 6 ? 6 + 2 * i : 38 + 2 * (i - 6); }
constexpr int w2_pos(int i) { return i < 38 ? (i + 32) % 38 : (i - 6) % 39; }

__global__ void __launch_bounds__(512, 2) fwd(Params p) {
  extern __shared__ __attribute__((aligned(16))) unsigned char smem[];
  u64 pc0; asm volatile("s_getpc_b64 %0" : "=s"(pc0));
  const int w0 = __builtin_amdgcn_readfirstlane(threadIdx.x >> 6);
  u32 dc = 0;
  if (w0 == 7) touch(dc, (threadIdx.x & 63) < 19 ? (threadIdx.x & 63) * 128 : 0x7fffffffu, mk_rsrc((const void*)pc0, CODE_KB * KB), (blockIdx.x >> 3) * 19 * 128);
#define IDS() const int w = sfresh(w0), bid = sfresh(blockIdx.x), grp = (bid & 6) * 2 + (bid >> 6), m = (bid & 1) * 8 + ((bid >> 3) & 7), cu = grp * 16 + m, hw = w & 3, cb = (m >> 3) * 4 + (m & 3), th = (m >> 2) & 1, \
              rt = w & 1, kh = (w >> 1) & 1, T13 = 4 * grp + hw, T24 = 4 * grp + 2 * th + rt, rot = (4 * (m & 1) + (grp & 3) + ((m >> 1) & 3)) & 7, rot2 = (2 * th + grp + cb) & 3, rot4 = (2 * (cb & 3) + 4 * th + (grp & 3)) % 7; (void)rot; (void)rot2; (void)rot4; (void)cu; (void)cb; (void)kh; (void)T13; (void)T24
  const v4i rws = mk_rsrc(p.ws, WS_END);
  const u32 lane0 = threadIdx.x & 63;
  STAMP(); STAMPCLK(0);
  if (threadIdx.x < 4) ldsw<u32>(L_FLG + threadIdx.x * 4, 0u);
  BARRIER();
  v2u tagv;
  auto load_tags = [&]() { IDS(); const u32 g = grp + (lane0 < 3 ? lane0 : 0); bld8<0, true>(tagv, (g < 16 ? g : grp) * 256, rws, WS_EPOCH); };
  auto fix_tags = [&]() { pin(tagv); tagv = __builtin_bit_cast(v2u, __builtin_bit_cast(u64, tagv) + 1); };
  auto tag0 = [&]() { return (v2u){(u32)__builtin_amdgcn_readfirstlane(tagv.x), (u32)__builtin_amdgcn_readfirstlane(tagv.y)}; };
  auto publish = [&](int sid) { IDS(); const u32 lane = fresh(lane0); if (lane < 16) bst8(tag0(), lane * 128 + m * 8, rws, WS_FLAG + (sid * 256 + grp * 16) * 128); };
  auto await = [&](int sid, u32 first, u32 mask) {
    IDS(); const u64 t0 = __builtin_bit_cast(u64, tag0()); const u32 vo = (fresh(lane0) & mask) * 8;
#pragma unroll 1
    for (u32 spins = 0; spins < SPIN_CAP; ++spins) {
      v2u f; bld8<0, true>(f, vo, rws, WS_FLAG + (sid * 256 + cu) * 128 + first * 8); wait_vm<0>(f);
      if (__all(__builtin_bit_cast(u64, f) == t0)) break;
      __builtin_amdgcn_s_sleep(2);
    }
  };
  auto flg_wait = [&](bool q, u32 c) {
#pragma unroll 1
    for (u32 spins = 0; spins < SPIN_CAP; ++spins) { const v4u f = *(volatile LAS v4u*)(size_t)L_FLG; if ((u32)__builtin_amdgcn_readfirstlane(q ? f.x : min(f.y, f.z)) > c) break; if (w0 >= 4) __builtin_amdgcn_s_sleep(2); }
    asm volatile("" ::: "memory");
  };
  const u32 wrow = lane0 >> 2, wch = (lane0 & 3) * 16;
  if (w0 >= 4) {
    const v4i ro = mk_rsrc(p.wo, 512 * KB), r1 = mk_rsrc(p.w1, 1792 * KB), r2 = mk_rsrc(p.w2, 1792 * KB);
    u32 vo24, vo1;
    { IDS(); vo24 = wrow * 1024 + wch + cb * 128; vo1 = wrow * 3584 + wch + m * 256; }
    auto stage_w1 = [&](int s0, int s1) {
      IDS();
      if (m >= 14) return;
#pragma unroll 1
      for (int s = s0 + hw; s < s1; s += 4) {
        const u32 so = ((s + 17 + 4 * rot) & 31) * 16 * 3584;
        dma16(L_W1 + s * 4096, vo1, r1, so); dma16(L_W1 + s * 4096 + 1024, vo1, r1, so + 64); dma16(L_W1 + s * 4096 + 2048, vo1, r1, so + 128); dma16(L_W1 + s * 4096 + 3072, vo1, r1, so + 192);
      }
    };
    auto w2_row = [&](int i, int rot4) { const int t = (i * 37) >> 9, r = i - 14 * t, j = (r >> 1) + rot4; return (u32)(((j >= 7 ? j - 7 : j) * 8 + 2 * t + (r & 1)) * 16384); };
    auto stage_w2 = [&](int i0, int i1, int p0) {
      IDS();
#pragma unroll 1
      for (int i = i0 + hw; i < i1; i += 4) {
        const u32 la = (i + p0) * 4096, so = w2_row(i, rot4);
        dma16(la, vo24, r2, so); dma16(la + 1024, vo24, r2, so + 64); dma16(la + 2048, vo24, r2, so + 56 * 16384); dma16(la + 3072, vo24, r2, so + 56 * 16384 + 64);
      }
    };
    const v4u ones = {0x3f803f80u, 0x3f803f80u, 0x3f803f80u, 0x3f803f80u}, empt = {EMPTY, EMPTY, EMPTY, EMPTY};
    auto tables = [&]() {
      IDS(); const u32 lane = fresh(lane0), l16 = lane * 16; const int dq = (int)(lane & 31) - 4 * (int)(lane >> 5);
      sfor<4>([&](auto g) { f4 t; sfor<4>([&](auto i) { t[(int)i] = 8 * g + i <= dq ? 0.0f : -__builtin_inff(); }); ldsw<f4>(L_TA + g * 1024 + l16, t); });
      sfor<2>([&](auto g) { v4u b; sfor<4>([&](auto d) { const int c = 16 * g + 8 * (d >> 1) + 2 * (d & 1); b[(int)d] = (c > dq ? 0xffffu : 0u) | (c + 1 > dq ? 0xffff0000u : 0u); });
                            ldsw<v4u>(L_TB + g * 1024 + l16, b); });
      sfor<6>([&](auto g) { ldsw<v4u>(L_ONES + g * 1024 + l16, grp + g / 2 >= 2 ? ones : (v4u){}); });
    };
    auto cs_write = [&](const f16v& a, u32 dst) {
      const u32 lane = fresh(lane0);
      if ((lane & 31) == 0) sfor<4>([&](auto g) { v4u q; sfor<4>([&](auto i) { const float v = a[4 * g + i]; const u32 hi = pk(v, v) & 0xffffu; q[(int)i] = hi | (pk(0.0f, v - blo(hi)) & 0xffff0000u); });
                                                  ldsw<v4u>(dst + (8 * g + 4 * (lane >> 5)) * 4, q); });
    };
    auto put_stats = [&](f2 a, f2 b, u32 tok2) { ldsw<f4>(L_SD + tok2 * 16, (f4){a.x, a.y, b.x, b.y}); ldsw<f2>(L_RS + tok2 * 8, (f2){__builtin_amdgcn_rcpf(a.y), __builtin_amdgcn_rcpf(b.y)}); };
    v4u keep[24], w2r[30];
    {
      IDS();
      const u32 l16 = lane0 * 16;
      if (hw < 3) {
        const v4i rq = mk_rsrc(p.wqkv, 1536 * KB);
        const u32 vo = (8 * ((wrow >> 2) & 1) + 4 * (wrow >> 3) + (wrow & 3)) * 3072 + wch + m * 64 + hw * 1024;
        auto chunk = [&](int c) { const u32 so = 64 * ((c + rot) & 7) * 3072, la = L_WQ + c * 12288 + hw * 1024; sfor<4>([&](auto i) { dma16(la + i * 3072, vo, rq, so + i * 16 * 3072); }); };
        if (hw == 0) flg_wait(false, QSTART);
        load_tags();
        chunk(0);
        if (hw != 0) __builtin_amdgcn_s_sleep(KVDELAY);
        chunk(1); chunk(2); chunk(3);
        f16v a = {}; const u32 b0 = L_WQ + hw * 1024 + trbase(lane0);
        auto landed = [&](int c) {
          __builtin_amdgcn_s_setprio(3);
          ldsw<u32>(L_FLG + hw * 4, c + 1);
          v4u q[4]; sfor<4>([&](auto i) { q[i] = trfrag(b0 + (4 * c + i) * 3072); }); SBAR();
          sfor<4>([&](auto i) { a = mfma(q[i], ones, a); });
        };
#pragma unroll 1
        for (int c = 0; c < 4; ++c) { WAIT_VM(12); ldsw<u32>(L_FLG + hw * 4, c + 1); if (!LPRIO) __builtin_amdgcn_s_setprio(0); chunk(c + 4); landed(c); }
        WAIT_VM(12); landed(4); WAIT_VM(8); landed(5); WAIT_VM(4); landed(6); WAIT_VM(0); landed(7);
        cs_write(a, L_CSQ + hw * 128);
        asm volatile("s_waitcnt lgkmcnt(0)" ::: "memory");
        ldsw<u32>(L_FLG + hw * 4, 9);
        __builtin_amdgcn_s_setprio(0);
        fix_tags();
        if (hw == 1 && m < 14) { const u32 v = ((const GAS u32*)p.b1)[m * 64 + lane0]; ldsw<v2u>(L_B1 + lane0 * 8, (v2u){(v & 0xffffu) | (v << 16), (v >> 16) | (v & 0xffff0000u)}); }
      } else {
        u32 dd[4];
        touch(dd[0], lane0 < 48 ? lane0 * 128 : 0x7fffffffu, mk_rsrc(p.wqkv, 1536 * KB), bid * 6144);
        touch(dd[1], lane0 * 128, mk_rsrc(p.x, 2 * MB), (grp * 128 + m * 8) * 1024);
        load_tags();
        tables();
        WAIT_VM(0); fix_tags(); pin(dd[0]); pin(dd[1]); pin(dc);
        flg_wait(false, 5);
        touch(dd[0], lane0 < 16 ? lane0 * 256 * 128 : 0x7fffffffu, rws, WS_FLAG + cu * 128);
        touch(dd[1], lane0 < 56 ? lane0 * 128 : 0x7fffffffu, r1, bid * 7168); touch(dd[3], lane0 < 16 ? lane0 * 128 : 0x7fffffffu, ro, bid * 2048); touch(dd[2], lane0 < 56 ? lane0 * 128 : 0x7fffffffu, r2, bid * 7168);
        WAIT_VM(0); pin(dd[0]); pin(dd[1]); pin(dd[2]); pin(dd[3]);
      }
      if (STAMPL) { STAMP(); STAMP(); }
    }
    sfor<2>([&](auto L_) {
      constexpr int L = L_;
      IDS();
      const u32 lane = fresh(lane0), l16 = lane * 16;
      auto gather_stats = [&](int buf) {
        __builtin_amdgcn_s_setprio(3);
        v4u q[4]; const u32 so = WS_ST + buf * 256 * KB + T13 * 4096;
#pragma unroll 1
        for (u32 spins = 0; spins < SPIN_CAP; ++spins) {
          bld16<0, true>(q[0], l16, rws, so); bld16<1024, true>(q[1], l16, rws, so); bld16<2048, true>(q[2], l16, rws, so); bld16<3072, true>(q[3], l16, rws, so);
          wait_vm<0>(q[0], q[1]); wait_vm<0>(q[2], q[3]);
          if (!empty2(q[0], q[1]) && !empty2(q[2], q[3])) break;
          __builtin_amdgcn_s_sleep(4);
        }
        float s[4];
        sfor<4>([&](auto i) { const u32 a = q[0][(int)i], b = q[1][(int)i], c = q[2][(int)i], d = q[3][(int)i];
                              float v = fadd(fadd(bf(a), bf(b)), fadd(bf(c), bf(d)));
                              auto r = __builtin_amdgcn_permlane16_swap(fb(v), fb(v), false, false); const u32 r0 = r[0], r1 = r[1];
                              const f2 hh = halves(fadd(bf(r0), bf(r1))); s[i] = fadd(hh.x, hh.y); });
        if (lane < 16) put_stats(ln_stats(s[0], s[1]), ln_stats(s[2], s[3]), hw * 16 + lane);
        __builtin_amdgcn_s_setprio(0);
      };
      auto fill_x = [&](int buf) { const u32 so = WS_XF + buf * 2 * MB + (T24 * 32 + cb * 4 + kh * 2) * 1024; bst16<0>(empt, l16, rws, so); bst16<1024>(empt, l16, rws, so);
                                   if (lane < 16) bst16<0>(empt, l16, rws, WS_ST + buf * 256 * KB + (T24 * 16 + cb * 2 + kh) * 256); };
      auto fill_h = [&](auto i0, auto n) { if (m < 14) sfor<n>([&](auto j) { constexpr int i = i0 + j; bst16<(i & 3) * 1024>(empt, l16, rws, WS_HF + L * 7 * MB + (T13 * 112 + m * 8 + (i & 4)) * 1024); }); };
      auto stage_wo = [&](int s0, int s1) {
#pragma unroll 1
        for (int s = s0 + hw; s < s1; s += 4) { const u32 so = ((s & 16) + ((s + 4 * rot2) & 15)) * 16384; dma16(L_WO + s * 2048, vo24, ro, so); dma16(L_WO + s * 2048 + 1024, vo24, ro, so + 64); } };
      auto w2_get = [&](v4u* r, int s0) { const u32 so = w2_row(s0, rot4) + (hw & 1) * 16384 + (hw >> 1) * 56 * 16384; bld16<0, false>(r[0], vo24, r2, so); bld16<64, false>(r[1], vo24, r2, so); };
      auto w2_set = [&](v4u* r, int s0, u32 ta) { pin(r[0]); pin(r[1]); const u32 o = ta + ((hw & 1) ? w2_pos(s0 + 1) : w2_pos(s0)) * 4096; ldsw<v4u>(o, r[0]); ldsw<v4u>(o + 1024, r[1]); };
      auto w2_regs = [&](auto i0, auto n) { sfor<n>([&](auto j) { constexpr int i = i0 + j; w2_get(&w2r[2 * i], w2r_slot(i)); }); };
      auto w2_put = [&](auto i0, auto n) { const u32 ta = fresh(l16 + (hw >> 1) * 2048); sfor<n>([&](auto j) { constexpr int i = i0 + j; w2_set(&w2r[2 * i], w2r_slot(i), ta); }); };
      v4u st1[24];
      auto w1_pos = [&](int i) { const int j = hw + 4 * i; return j < 11 ? j : j == 11 ? 23 : j < 16 ? j - 1 : j + 8; };
      auto w1_get = [&](auto i0, auto n) { if (m < 14) sfor<n>([&](auto j) { constexpr int i = i0 + j; const u32 so = ((w1_pos(i) + 17 + 4 * rot) & 31) * 16 * 3584;
                                                                             sfor<4>([&](auto t) { bld16<t * 64, false>(st1[4 * i + t], vo1, r1, so); }); }); };
      auto w1_get_rw = [&]() { if (m < 14) sfor<3>([&](auto j) { constexpr int i = 3 + j; const u32 so = ((w1_pos(i) + 17 + 4 * rot) & 31) * 16 * 3584;
                                                                   sfor<4>([&](auto t) { bld16<t * 64, false>(st1[4 * i + t], vo1, r1, so); }); }); };
      auto w1_put = [&](auto i0, auto n) { if (m < 14) sfor<n>([&](auto j) { constexpr int i = i0 + j; const u32 o = fresh(l16) + L_W1 + w1_pos(i) * 4096;
                                                                             sfor<4>([&](auto t) { pin(st1[4 * i + t]); ldsw<v4u>(o + t * 1024, st1[4 * i + t]); }); }); };
      f16v cs = {};
      auto w1_colsum = [&](int p0, int p1) {
        const u32 b0 = L_W1 + hw * 1024 + trbase(lane);
#pragma unroll 1
        for (int z = p0; z < p1; z += 4) { v4u q[4]; sfor<4>([&](auto i) { q[i] = trfrag(b0 + (z + i < p1 ? z + i : p0) * 4096); }); SBAR();
                                           sfor<4>([&](auto i) { cs = mfma(q[i], z + i < p1 ? ones : (v4u){}, cs); }); }
      };
      auto halo = [&]() {
        __builtin_amdgcn_s_setprio(3);
        if (4 * grp + 2 * hw >= 8) {
          await(L, 2 * hw, 1);
          sfor<8>([&](auto i) { dma16<true>(L_KV + hw * 8192 + i * 1024, l16, rws, WS_KV + L * 4 * MB + ((4 * grp - 8 + 2 * hw + (i >> 2)) * 16 + m) * 4096 + (i & 3) * 1024); });
        } else sfor<8>([&](auto i) { ldsw<v4u>(L_KV + hw * 8192 + i * 1024 + l16, (v4u){}); });
        __builtin_amdgcn_s_setprio(0);
      };
      auto hint = [&](int sid) { if (hw == 3) { __builtin_amdgcn_s_setprio(3); publish(sid); __builtin_amdgcn_s_setprio(0); } };
      v2u hf;
      auto ask = [&](int sid) { if (hw == 2) bld8<0, true>(hf, (lane & 15) * 8, rws, WS_FLAG + (sid * 256 + cu) * 128); };
      auto hinted = [&](int sid) { if (hw == 2) { __builtin_amdgcn_s_setprio(3); wait_vm<0>(hf); if (!__all(__builtin_bit_cast(u64, hf) == __builtin_bit_cast(u64, tag0()))) await(sid, 0, 15); __builtin_amdgcn_s_setprio(0); } };
      if constexpr (L == 1) HBAR();
      if (L == 1 && hw == 3) tables();
      if (L == 1 || hw != 0) w1_get(ic<3>{}, ic<3>{});
      HBAR();
      fill_h(ic<0>{}, ic<2>{});
      if (L == 1) __builtin_amdgcn_s_sleep(6);
      else sfor<24>([&](auto i) { keep[i] = ldsr<v4u>(L_WQ + hw * 24 * KB + i * 1024 + l16); });
      halo();
      fill_x(2 * L);
      WAIT_VM(3); BARRIER();
      if (L == 0 && hw == 0) w1_get_rw();
      if (L == 0) { const u32 so = WS_OF + (T13 * 32 + 2 * m) * 1024; bst16<0>(empt, l16, rws, so); bst16<1024>(empt, l16, rws, so); }
      stage_wo(0, 32);
      stage_w1(15, 23);
      w1_get(ic<0>{}, ic<3>{});
      WAIT_VM(0);
      HBAR();
      hint(3 + L);
      fill_h(ic<2>{}, ic<2>{});
      if (L == 0) w2_regs(ic<0>{}, ic<3>{});
      BARRIER();
      ask(3 + L);
      w1_put(ic<0>{}, ic<2>{});
      if (hw < 3) w1_put(ic<2>{}, ic<1>{});
      hinted(3 + L);
      BARRIER();
      if (L == 0 && m < 14) w1_colsum(0, 11);
      HBAR();
      hint(5 + L);
      fill_h(ic<4>{}, ic<2>{});
      if (L == 0 && m < 14) w1_colsum(15, 19);
      BARRIER();
      ask(5 + L);
      w1_put(ic<4>{}, ic<2>{});
      if (hw == 3) w1_put(ic<2>{}, ic<1>{});
      hinted(5 + L);
      stage_w2(0, 6, 32);
      if (L == 0) w2_regs(ic<3>{}, ic<1>{});
      BARRIER();
      gather_stats(2 * L);
      fill_h(ic<6>{}, ic<2>{});
      w1_put(ic<3>{}, ic<1>{});
      if (L == 0 && m < 14) w1_colsum(19, 32);
      WAIT_VM(0);
      HBAR();
      hint(8 + L);
      if (L == 0 && m < 14) { __builtin_amdgcn_s_setprio(3); w1_colsum(11, 15); cs_write(cs, L_CS1 + hw * 128); __builtin_amdgcn_s_setprio(0); }
      HBAR();
      if (L == 0) w2_regs(ic<4>{}, ic<2>{});
      stage_w2(21, 37, -6);
      ask(8 + L); hinted(8 + L);
      if (L == 0) { w2_regs(ic<6>{}, ic<9>{}); WAIT_VM(34); }
      BARRIER();
      stage_w2(hw < 3 ? 18 : 34, hw < 3 ? 21 : 38, -6);
      w2_put(ic<0>{}, ic<6>{});
      BARRIER();
      if (L == 0) { fill_x(1); const u32 so = WS_OF + 2 * MB + (T13 * 32 + 2 * m) * 1024; bst16<0>(empt, l16, rws, so); bst16<1024>(empt, l16, rws, so); WAIT_VM(5); }
      else WAIT_VM(0);
      HBAR();
      HBAR();
      w2_put(ic<6>{}, ic<9>{});
      HBAR();
      HBAR();
      const u32 ka = fresh(L_WQ + hw * 24 * KB + (8 * ((lane >> 4) & 1) + 4 * (lane >> 5) + ((lane >> 2) & 3)) * 64 + (lane & 3) * 16);
      if (L == 0 && hw < 2) sfor<12>([&](auto i) { ldsw<v4u>(ka + i * 1024, keep[i]); });
      WAIT_VM(0);
      HBAR();
      if (L == 0) hint(7);
      if (L == 0 && hw < 2) sfor<12>([&](auto i) { ldsw<v4u>(ka + (12 + i) * 1024, keep[12 + i]); });
      BARRIER();
      if constexpr (L == 0) {
        ask(7);
        if (hw >= 2) sfor<24>([&](auto i) { ldsw<v4u>(ka + i * 1024, keep[i]); });
        hinted(7);
        BARRIER();
        gather_stats(1);
      }
    });
    STAMP();
    return;
  }
  if (CPRIO) __builtin_amdgcn_s_setprio(CPRIO);
  v4u res[2], b2p[2];
  v4u qf[2];
  struct Aux { v4u tf; u32 c[4]; float rs; f2 st; f4 rv[4]; };
  auto aux_read = [&](Aux& x, bool ffn) {
    IDS();
    const u32 lane = fresh(lane0), h = lane >> 5, t32 = lane & 31;
    x.st = ldsr<f2>(L_SD + hw * 256 + t32 * 8);
    x.rs = ldsr<float>(L_RS + hw * 128 + t32 * 4);
    if (ffn) { const u32 lc = fresh(L_CS1 + h * 512 + t32 * 4); sfor<4>([&](auto t) { x.c[t] = ldsr<u32>(lc + t * 128); }); }
    else { const u32 lc = fresh(L_CSQ + t32 * 4), lr = fresh(L_RS + hw * 128 + 16 * h);
           sfor<3>([&](auto t) { x.c[t] = ldsr<u32>(lc + t * 128); });
           sfor<4>([&](auto g) { x.rv[g] = ldsr<f4>(lr + 32 * g); }); }
  };
  auto aux_make = [&](Aux& x, bool ffn) { const u32 h = fresh(lane0) >> 5; x.tf = aux_tok(x.st, h); if (!ffn && h) x.tf = (v4u){}; pin(x.tf); };
  v4u kv[4]; u32 lk;
  auto kv_epilogue = [&](u32 so, const Aux& x, f16v& ak, f16v& av) {
    const u32 l16 = fresh(lane0) * 16;
    ak *= x.rs;
    kv[0] = pk8(ak, 0); kv[1] = pk8(ak, 8); bst16<0>(kv[0], l16, rws, so); bst16<1024>(kv[1], l16, rws, so);
    SBAR();
    sfor<16>([&](auto r) { av[(int)r] *= x.rv[r / 4][r % 4]; });
    kv[2] = pk8(av, 0); kv[3] = pk8(av, 8); bst16<2048>(kv[2], l16, rws, so); bst16<3072>(kv[3], l16, rws, so);
    lk = fresh(L_KV + (8 + w0) * 4096 + l16);
  };
  auto q_epilogue = [&](const Aux& x, f16v aq) { aq *= x.rs * (0.25f * 1.4426950408889634f); qf[0] = pk8(aq, 0); qf[1] = pk8(aq, 8); };
  u32 kvf; { IDS(); kvf = (lane0 == 1 || lane0 == 2) && grp + lane0 < 16 ? WS_FLAG + cu * 128 + lane0 * (16 * 128 - 32) + 64 + hw * 8 : 0x7fffffffu; }
  auto kv_flag = [&](int L) { WAIT_VM(0); bst8(tagv, kvf, rws, L * 256 * 128); };
  {
    IDS();
    const u32 lane = lane0, h = lane >> 5, t32 = lane & 31;
    const v4i rx = mk_rsrc(p.x, 2 * MB);
    const u32 vo = wrow * 1024 + (((lane & 3) ^ ((lane >> 4) & 3)) * 16), so = T13 * 32 * 1024, xs = hw * 12 * KB;
    auto issue = [&](int u) { const u32 o = u < 16 ? so + ((u + 2 * rot) & 15) * 64 : T24 * 32 * 1024 + cb * 128 + kh * 64;
                              dma16(xs + (u % 6) * 2048, vo, rx, o); dma16(xs + (u % 6) * 2048 + 1024, vo, rx, o + 16384); };
    sfor<6>([&](auto u) { issue(u); });
    const u32 xa0 = xs + t32 * 64 + ((h ^ ((t32 >> 2) & 3)) * 16), xa1 = xa0 ^ 32;
    v4u xq[32]; v2u bb[4];
    float s1 = 0.f, s2 = 0.f;
    sfor<17>([&](auto u) {
      if constexpr (u < 16) {
        asm volatile("s_waitcnt vmcnt(%0)" ::"i"(2 * ((u == 0 ? 5 : u + 4 < 16 ? u + 4 : 16) - u) + (u > 12 ? 5 : 0)) : "memory");
        xq[2 * u] = ldsr<v4u>(xa0 + (u % 6) * 2048); xq[2 * u + 1] = ldsr<v4u>(xa1 + (u % 6) * 2048);
        SBAR();
      }
      if constexpr (u > 0) {
        pin(xq[2 * u - 2]); pin(xq[2 * u - 1]);
        if constexpr (u + 5 < 17) issue(u + 5);
        if constexpr (u == 12) { load_tags(); const v4i rb = mk_rsrc(p.b2, 1024); const u32 bo = (cb * 64 + kh * 32 + 4 * h) * 2; sfor<4>([&](auto i) { bld8<i * 16, false>(bb[i], bo, rb, 0); }); }
        sfor<8>([&](auto i) { const u32 eu = xq[2 * u - 2 + i / 4][i % 4]; const bf2 e = __builtin_bit_cast(bf2, eu);
                              s1 = __builtin_amdgcn_fdot2_f32_bf16(e, __builtin_bit_cast(bf2, 0x3f803f80u), s1, false); s2 = __builtin_amdgcn_fdot2_f32_bf16(e, e, s2, false); });
        pin(s1); pin(s2);
      }
      SBAR();
    });
    { const f2 st = ln_stats(hsum(s1), hsum(s2)); ldsw<f2>(L_SD + hw * 256 + t32 * 8, st); ldsw<float>(L_RS + hw * 128 + t32 * 4, __builtin_amdgcn_rcpf(st.y)); }
    WAIT_VM(0);
    fix_tags();
    sfor<4>([&](auto i) { pin(bb[i]); });
    sfor<2>([&](auto f) { const u32 ra = xs + (16 % 6) * 2048 + t32 * 64 + 8 * h, z = ((t32 >> 2) & 3) * 16;
                          const v2u a = ldsr<v2u>(ra + ((2 * f * 16) ^ z)), b = ldsr<v2u>(ra + (((2 * f + 1) * 16) ^ z));
                          res[f] = (v4u){a.x, a.y, b.x, b.y}; b2p[f] = (v4u){bb[2 * f].x, bb[2 * f].y, bb[2 * f + 1].x, bb[2 * f + 1].y}; pin(res[f]); });
    const u32 wb0 = fresh(L_WQ + trbase(lane)), wb1 = fresh(wb0 + 48 * KB);
    auto addr = [&](auto i) { constexpr int o = (i / 2) * 3072 + 1024 + (i & 1) * 1024; return o < 48 * KB ? wb0 + o : wb1 + (o - 48 * KB); };
    auto addrq = [&](auto i) { constexpr int o = i * 3072; return o < 48 * KB ? wb0 + o : wb1 + (o - 48 * KB); };
    f16v acc1[2] = {};
    if (STAMPL) STAMP();
    flg_wait(false, 1);
    gemm<2, 32, 0, 32, true, false>(acc1, xq, 0, addr, [&](auto i) { constexpr int n = i + DEP; if constexpr (n == 16 || n == 32 || n == 48 || n == 56) { pin(acc1[0]); pin(acc1[1]); flg_wait(false, n / 8 + (n < 48)); } } RETRY_PASS);
    pin(acc1[0]); pin(acc1[1]);
    flg_wait(false, 8);
    if (STAMPL) STAMP();
    Aux ax; aux_read(ax, false);
    SBAR();
    aux_make(ax, false);
    acc1[0] = mfma(aux_col(ax.c[1]), ax.tf, acc1[0]); acc1[1] = mfma(ax.tf, aux_col(ax.c[2]), acc1[1]);
    kv_epilogue(WS_KV + (T13 * 16 + m) * 4096, ax, acc1[0], acc1[1]);
    flg_wait(true, 7);
    f16v accq = {};
    gemm<1, 32, 0, 32, false, false>(&accq, xq, 0, addrq, [&](auto i) {
      if constexpr (i == 14) { kv_flag(0); LBAR(); ax.c[0] = ldsr<u32>(L_CSQ + t32 * 4); }
      if constexpr (i % 4 == 0 && i >= 16) ldsw<v4u>(lk + (i / 4 - 4) * 1024, kv[i / 4 - 4]);
      if constexpr (i == 22) accq = mfma(aux_col(ax.c[0]), ax.tf, accq); } RETRY_PASS);
    q_epilogue(ax, accq);
  }
  for (int L = 0;; ++L) {
    IDS();
    const u32 lane = fresh(lane0), l16 = lane * 16, h = lane >> 5, t32 = lane & 31;
    auto res_init = [&](f16v (&acc)[2], bool bias) {
      sfor<8>([&](auto i) { const u32 r = res[i / 4][i % 4], b = bias ? fresh(b2p[i / 4][i % 4]) : 0u;
                            acc[0][2 * i] = bias ? blo(r) + blo(b) : blo(r); acc[0][2 * i + 1] = bias ? bhi(r) + bhi(b) : bhi(r); });
      acc[1] = (f16v){};
      pin(acc[0]);
    };
    auto res_epilogue = [&](f16v (&acc)[2], int buf, bool last) {
      const u32 xa = fresh(L_XCH + w * 4096 + l16), xb = fresh(L_XCH + (w ^ 2) * 4096 + l16);
      const u32 so = sfresh(WS_XF + buf * 2 * MB + (T24 * 32 + cb * 4 + kh * 2) * 1024), ss = sfresh(WS_ST + buf * 256 * KB + (T24 * 16 + cb * 2 + kh) * 256);
      sfor<4>([&](auto g) { ldsw<f4>(xa + g * 1024, (f4){acc[1][4 * g], acc[1][4 * g + 1], acc[1][4 * g + 2], acc[1][4 * g + 3]}); });
      BARRIER();
      f4 o[4]; sfor<4>([&](auto g) { o[g] = ldsr<f4>(xb + g * 1024); });
      f16v v; sfor<16>([&](auto r) { v[(int)r] = acc[0][(int)r] + o[r / 4][r % 4]; });
      res[0] = canon(pk8(v, 0)); res[1] = canon(pk8(v, 8));
      if (!last) {
        bst16<0>(res[0], l16, rws, so); bst16<1024>(res[1], l16, rws, so);
        SBAR();
        float s1 = 0.f, s2 = 0.f;
        sfor<8>([&](auto i) { const u32 eu = res[i / 4][i % 4]; const bf2 e = __builtin_bit_cast(bf2, eu);
                              s1 = __builtin_amdgcn_fdot2_f32_bf16(e, __builtin_bit_cast(bf2, 0x3f803f80u), s1, false); s2 = __builtin_amdgcn_fdot2_f32_bf16(e, e, s2, false); });
        s1 = hsum(s1); s2 = hsum(s2);
        bst8((v2u){canon(fb(s1)), canon(fb(s2))}, t32 * 8, rws, ss);
        WAIT_VM(0);
        BARRIER();
      } else {
        GAS unsigned char* op = (GAS unsigned char*)p.out + (T24 * 32 + t32) * 1024 + (cb * 64 + kh * 32 + 8 * h) * 2;
        sfor<2>([&](auto f) {
          auto a = __builtin_amdgcn_permlane32_swap(res[f].x, res[f].z, false, false), b = __builtin_amdgcn_permlane32_swap(res[f].y, res[f].w, false, false);
          if (OUTSC) __builtin_nontemporal_store((v4u){a[0], b[0], a[1], b[1]}, (GAS v4u*)(op + f * 32)); else *(GAS v4u*)(op + f * 32) = (v4u){a[0], b[0], a[1], b[1]}; });
      }
    };
    {
      Attn at; at.init(lane, hw); at.q[0] = qf[0]; at.q[1] = qf[1];
      at.diag();
      BARRIER();
      at.rest([&]() { LBAR();   });
      const u32 so = WS_OF + L * 2 * MB + (T13 * 32 + 2 * m) * 1024; const int jmin = max(8 - T13, 0);
      bst16<0>(at.finish<0>(jmin), l16, rws, so); bst16<1024>(at.finish<1>(jmin), l16, rws, so);
    }
    BARRIER();
    WAIT_VM(0);
    BARRIER();
    {
      constexpr int LOOK = 10;
      v4u win[LOOK];
      f16v acc2[2];
      const u32 so = WS_OF + L * 2 * MB + (T24 * 32 + kh * 16) * 1024, wb = fresh(L_WO + kh * 33 * KB + trbase(lane)), wc = fresh(wb ^ 1024);
      auto issue = [&](auto i, v4u& d) { bld16<(i & 3) * 1024, true>(d, l16, rws, so + (((i >> 2) + rot2) & 3) * 4096); };
      auto addr = [&](auto i) { return ((i & 1) ? wc : wb) + (i / 2) * 2048; };
      sfor<LOOK>([&](auto i) { issue(i, win[i]); });
      res_init(acc2, false);
      gemm<2, 16, LOOK, LOOK, false, true>(acc2, win, issue, addr, [&](auto i) { if constexpr (i == 16) LBAR();   } RETRY_PASS);
      res_epilogue(acc2, 2 * L, false);
    }
    const u32 soh = WS_HF + L * 7 * MB + (T24 * 112 + kh * 56) * 1024;
    constexpr int LOOK4 = LK4;
    v4u hw4[LOOK4];
    u32 pb[7];
    auto h_bases = [&]() { sfor<7>([&](auto j) { const int jj = j + rot4; pb[j] = soh + (jj >= 7 ? jj - 7 : jj) * 8192; }); };
    if (m < 14) {
      constexpr int LOOK = 22;
      v4u win[LOOK];
      f16v acc3[4] = {};
      const u32 so = WS_XF + 2 * L * 2 * MB + T13 * 32 * 1024, wb0 = fresh(L_W1 + trbase(lane)), wb1 = fresh(wb0 + 63 * KB), wb2 = fresh(wb0 + 126 * KB);
      const u32 sh = WS_HF + L * 7 * MB + (T13 * 112 + m * 8) * 1024;
      auto issue = [&](auto i, v4u& d) { bld16<(i & 3) * 1024, true>(d, l16, rws, so + (((i >> 2) + rot) & 7) * 4096); };
      auto addr = [&](auto i) { constexpr int o = ((i + 60) & 127) * 1024; return o < 63 * KB ? wb0 + o : o < 126 * KB ? wb1 + (o - 63 * KB) : wb2 + (o - 126 * KB); };
      sfor<8>([&](auto i) { issue(i, win[i]); });
      Aux ax;
      constexpr float K1 = -2.0f * 0.7978845608028654f * 1.4426950408889634f, K2 = K1 * 0.044715f;
      float c1, c2, za, zb; v4u q;
      auto gelu_stage = [&](auto e, auto g) {
        const float ua = acc3[e / 8][2 * (e % 8)], ub = acc3[e / 8][2 * (e % 8) + 1], sd = ax.st.y;
        if constexpr (g == 0) { za = __builtin_fmaf(ua * ua, c2, c1); zb = __builtin_fmaf(ub * ub, c2, c1); }
        if constexpr (g == 1) { za = ex2(za * ua); zb = ex2(zb * ub); }
        if constexpr (g == 2) { za = __builtin_amdgcn_rcpf(__builtin_fmaf(za, sd, sd)); zb = __builtin_amdgcn_rcpf(__builtin_fmaf(zb, sd, sd)); }
        if constexpr (g == 3) { za *= ua; zb *= ub; }
        pin(za); pin(zb);
        if constexpr (g == 3) { u32 r = canon(pk(za, zb)); pin(r); q[e % 4] = r; if constexpr (e % 4 == 3) bst16<((e / 4) & 3) * 1024>(q, l16, rws, sh); }
      };
      gemm<4, 32, LOOK, LOOK, false, true, 8, 16>(acc3, win, issue, addr, [&](auto i) {
        if constexpr (i == 40) LBAR();
        if constexpr (i == 63) { LBAR(); aux_read(ax, true); }
        if constexpr (i == 68) { aux_make(ax, true); c1 = K1 * ax.rs; c2 = K2 * ax.rs * ax.rs * ax.rs; pin(c1); pin(c2); }
        if constexpr (i >= 72 && i < 76) acc3[i & 3] = mfma(aux_col(ax.c[i & 3]), ax.tf, acc3[i & 3]);
        if constexpr (i >= 80) gelu_stage(ic<(i - 80) / 4>{}, ic<i % 4>{});
      } RETRY_PASS);
      BARRIER();
      sfor<5>([&](auto f_) {
        constexpr int f = 3 + f_;
        sfor<4>([&](auto d) {
          const f2 u = {acc3[f / 2][(f & 1) * 8 + 2 * d], acc3[f / 2][(f & 1) * 8 + 2 * d + 1]}, sd = {ax.st.y, ax.st.y};
          const f2 z = u * __builtin_elementwise_fma(u * u, (f2){c2, c2}, (f2){c1, c1});
          const f2 dn = __builtin_elementwise_fma((f2){ex2(z.x), ex2(z.y)}, sd, sd);
          const f2 g = u * (f2){__builtin_amdgcn_rcpf(dn.x), __builtin_amdgcn_rcpf(dn.y)};
          q[(int)d] = canon(pk(g.x, g.y));
        });
        bst16<(f & 3) * 1024>(q, l16, rws, sh + (f >> 2) * 4096);
        SBAR();
        if constexpr (f == 3) h_bases();
        if constexpr (f >= 4) {
          sfor<LOOK4 / 4>([&](auto j) { constexpr int i = LOOK4 / 4 * (f - 4) + j, k = w2_kstep(i); bld16<(k & 3) * 1024, true>(hw4[i], l16, rws, pb[k >> 3]); });
          SBAR();
        }
      });
    } else {
      LBAR(); LBAR(); BARRIER();
      h_bases();
#pragma unroll 1
      for (u32 spins = 0; spins < SPIN_CAP; ++spins) {
        __builtin_amdgcn_s_sleep(8);
        bld16<2048, true>(hw4[0], l16, rws, soh); bld16<3072, true>(hw4[1], l16, rws, soh); wait_vm<0>(hw4[0], hw4[1]);
        if (!empty2(hw4[0], hw4[1])) break;
      }
      sfor<LOOK4>([&](auto i) { constexpr int k = w2_kstep(i); bld16<(k & 3) * 1024, true>(hw4[i], l16, rws, pb[k >> 3]); });
    }
    {
      f16v acc2[2];
      const u32 wb0 = fresh(kh * 3072 + trbase(lane)), wb1 = fresh(wb0 + 60 * KB), wb2 = fresh(wb0 + 120 * KB), wc0 = fresh(wb0 ^ 1024), wc1 = fresh(wb1 ^ 1024), wc2 = fresh(wb2 ^ 1024);
      const u32 l16b = l16 + 4096;
      auto issue = [&](auto i, v4u& d) { constexpr int k = w2_kstep(i); bld16<(k & 3) * 1024, true>(d, (k & 4) ? l16b : l16, rws, pb[k >> 3]); };
      auto addr = [&](auto i) { constexpr int o = w2_pos(i / 2) * 4096; constexpr bool c = i & 1;
                                return o < 60 * KB ? (c ? wc0 : wb0) + o : o < 120 * KB ? (c ? wc1 : wb1) + (o - 60 * KB) : (c ? wc2 : wb2) + (o - 120 * KB); };
      res_init(acc2, true);
      BARRIER();
      gemm<2, 56, LOOK4, LOOK4, false, true>(acc2, hw4, issue, addr, [&](auto i) { if constexpr (i == 36 - DEP || i == 33 || i == 76 - DEP || i == 75 || i == 96) LBAR();   } RETRY_PASS);
      res_epilogue(acc2, 1, L == 1);
    }
    if (L == 1) break;
    {
      constexpr int LOOK = 12;
      const u32 so = WS_XF + 1 * 2 * MB + T13 * 32 * 1024, wb0 = fresh(L_WQ + trbase(lane)), wb1 = fresh(wb0 + 48 * KB);
      auto issue = [&](auto i, v4u& d) { bld16<(i & 3) * 1024, true>(d, l16, rws, so + (((i >> 2) + rot) & 7) * 4096); };
      auto addr = [&](auto i) { constexpr int o = (i / 2) * 3072 + 1024 + (i & 1) * 1024; return o < 48 * KB ? wb0 + o : wb1 + (o - 48 * KB); };
      auto addrq = [&](auto i) { constexpr int o = i * 3072; return o < 48 * KB ? wb0 + o : wb1 + (o - 48 * KB); };
      const u32 sk = sfresh(WS_KV + 4 * MB + (T13 * 16 + m) * 4096);
      f16v acc1[2] = {};
      v4u xq[32];
      sfor<LOOK>([&](auto i) { issue(i, xq[i]); });
      Aux ax;
      gemm<2, 32, LOOK, 32, true, true>(acc1, xq, issue, addr, [&](auto i) {
        if constexpr (i == 40) { LBAR(); aux_read(ax, false); }
        if constexpr (i == 48) aux_make(ax, false);
        if constexpr (i == 52) acc1[0] = mfma(aux_col(ax.c[1]), ax.tf, acc1[0]);
        if constexpr (i == 53) acc1[1] = mfma(ax.tf, aux_col(ax.c[2]), acc1[1]);
      } RETRY_PASS);
      kv_epilogue(sk, ax, acc1[0], acc1[1]);
      f16v accq = mfma(aux_col(ax.c[0]), ax.tf, (f16v){});
      gemm<1, 32, 0, 32, false, false>(&accq, xq, 0, addrq, [&](auto i) {
        if constexpr (i % 4 == 1 && i < 16) ldsw<v4u>(lk + (i / 4) * 1024, kv[i / 4]);
        if constexpr (i == KVI) { kv_flag(1); LBAR();   } } RETRY_PASS);
      q_epilogue(ax, accq);
    }
  }
  STAMP(); STAMPCLK(1);
  { IDS(); if (w == 0 && m == 0 && lane0 == 0) bst8(tagv, 0, rws, WS_EPOCH + grp * 256); }
}

extern "C" void kernel_launch(void* const* d_in, const int* in_sizes, int n_in, void* d_out, int out_size, void* d_ws, size_t ws_size, hipStream_t stream) {
  Params p;
  p.x = d_in[0]; p.wqkv = d_in[1]; p.wo = d_in[2]; p.w1 = d_in[4]; p.b1 = d_in[5]; p.w2 = d_in[6]; p.b2 = d_in[7]; p.out = d_out; p.ws = (unsigned char*)d_ws;
  (void)hipFuncSetAttribute((const void*)fwd, hipFuncAttributeMaxDynamicSharedMemorySize, LDS_BYTES);
  hipLaunchKernelGGL(fwd, dim3(256), dim3(512), LDS_BYTES, stream, p);
}
```

```cpp
#include <hip/hip_runtime.h>
#include <type_traits>
#include <utility>

#define CODE_KB 72
#define STAMPS 0

#define GAS __attribute__((address_space(1)))
#define LAS __attribute__((address_space(3)))
#define DI __device__ __forceinline__
typedef unsigned u32;
typedef unsigned long long u64;
typedef u32 v4u __attribute__((ext_vector_type(4)));
typedef u32 v2u __attribute__((ext_vector_type(2)));
typedef int v4i __attribute__((ext_vector_type(4)));
typedef short s4 __attribute__((ext_vector_type(4)));
typedef __bf16 bf8 __attribute__((ext_vector_type(8)));
typedef __bf16 bf2 __attribute__((ext_vector_type(2)));
typedef float f2 __attribute__((ext_vector_type(2)));
typedef float f4 __attribute__((ext_vector_type(4)));
typedef float f16v __attribute__((ext_vector_type(16)));
template <int I> using ic = std::integral_constant<int, I>;

template <class F, int... I> DI void sfor_impl(F&& f, std::integer_sequence<int, I...>) { (f(ic<I>{}), ...); }
template <int N, class F> DI void sfor(F&& f) { sfor_impl(f, std::make_integer_sequence<int, N>{}); }

constexpr int KB = 1024, MB = 1024 * 1024;
constexpr int WS_EPOCH = 0;
constexpr int WS_FLAG = 32 * KB;
constexpr int WS_XF = 1 * MB;
constexpr int WS_ST = WS_XF + 6 * MB;
constexpr int WS_KV = WS_ST + 1 * MB;
constexpr int WS_OF = WS_KV + 8 * MB;
constexpr int WS_HF = WS_OF + 4 * MB;
constexpr int WS_STAMP = WS_HF + 14 * MB;
constexpr int WS_END = WS_STAMP + (STAMPS ? MB : 0);
constexpr int L_KV = 0, L_ONES = 48 * KB, L_TA = 54 * KB, L_TB = 58 * KB, L_WQ = 60 * KB, L_WO = 92 * KB, L_W1 = 0, L_XCH = 44 * KB;
constexpr int L_SD = 156 * KB, L_RS = L_SD + 1024, L_CSQ = L_RS + 512, L_CS1 = L_CSQ + 384, L_B1 = L_CS1 + 512, L_FLG = L_B1 + 512, LDS_BYTES = 160 * KB;

struct Params { const void *x, *wqkv, *wo, *w1, *b1, *w2, *b2; void* out; unsigned char* ws; };

DI v4i mk_rsrc(const void* p, u32 bytes) { v4i r; r.x = (int)(u32)(u64)p; r.y = (int)((u32)((u64)p >> 32) & 0xffffu); r.z = (int)bytes; r.w = 0x00020000; return r; }
template <int OFF, bool SC1> DI void bld16(v4u& d, u32 voff, v4i rs, u32 soff) {
  if constexpr (SC1) asm volatile("buffer_load_dwordx4 %0, %1, %2, %3 offen offset:%4 sc1" : "=v"(d) : "v"(voff), "s"(rs), "s"(soff), "i"(OFF));
  else asm volatile("buffer_load_dwordx4 %0, %1, %2, %3 offen offset:%4" : "=v"(d) : "v"(voff), "s"(rs), "s"(soff), "i"(OFF));
}
template <int OFF, bool SC1> DI void bld8(v2u& d, u32 voff, v4i rs, u32 soff) {
  if constexpr (SC1) asm volatile("buffer_load_dwordx2 %0, %1, %2, %3 offen offset:%4 sc1" : "=v"(d) : "v"(voff), "s"(rs), "s"(soff), "i"(OFF));
  else asm volatile("buffer_load_dwordx2 %0, %1, %2, %3 offen offset:%4" : "=v"(d) : "v"(voff), "s"(rs), "s"(soff), "i"(OFF));
}
template <int OFF> DI void bst16(v4u d, u32 voff, v4i rs, u32 soff) {
  asm volatile("buffer_store_dwordx4 %0, %1, %2, %3 offen offset:%4 sc1\n\ts_nop 1" ::"v"(d), "v"(voff), "s"(rs), "s"(soff), "i"(OFF));
}
DI void bst8(v2u d, u32 voff, v4i rs, u32 soff) { asm volatile("buffer_store_dwordx2 %0, %1, %2, %3 offen sc1" ::"v"(d), "v"(voff), "s"(rs), "s"(soff)); }
DI void touch(u32& d, u32 voff, v4i rs, u32 soff) { asm volatile("buffer_load_dword %0, %1, %2, %3 offen" : "=v"(d) : "v"(voff), "s"(rs), "s"(soff)); }
template <bool SC1 = false> DI void dma16(u32 lds, u32 voff, v4i rs, u32 soff) {
  if constexpr (SC1) asm volatile("s_mov_b32 m0, %0\n\ts_nop 0\n\tbuffer_load_dwordx4 %1, %2, %3 offen sc1 lds" ::"s"(lds), "v"(voff), "s"(rs), "s"(soff));
  else asm volatile("s_mov_b32 m0, %0\n\ts_nop 0\n\tbuffer_load_dwordx4 %1, %2, %3 offen lds" ::"s"(lds), "v"(voff), "s"(rs), "s"(soff));
}
template <int N, class T> DI void wait_vm(T& x) { asm volatile("s_waitcnt vmcnt(%1)" : "+v"(x) : "i"(N)); }
template <int N, class T> DI void wait_vm(T& x, T& y) { asm volatile("s_waitcnt vmcnt(%2)" : "+v"(x), "+v"(y) : "i"(N)); }
template <class T> DI void pin(T& x) { asm volatile("" : "+v"(x)); }
#define WAIT_VM(n) asm volatile("s_waitcnt vmcnt(" #n ")" ::: "memory")
#define BARRIER() do { SBAR(); if (STAMPS != 3) STAMP(); asm volatile("s_waitcnt lgkmcnt(0)\n\ts_barrier" ::: "memory"); SBAR(); } while (0)
#define STAMPL 0
#define LBAR() do { SBAR(); if (STAMPL) STAMP(); asm volatile("s_barrier" ::: "memory"); SBAR(); } while (0)
#define HBAR() do { if (STAMPL) STAMP(); asm volatile("s_waitcnt lgkmcnt(0)\n\ts_barrier" ::: "memory"); } while (0)
#define SBAR() __builtin_amdgcn_sched_barrier(0)
DI f16v mfma(v4u a, v4u b, f16v c) { return __builtin_amdgcn_mfma_f32_32x32x16_bf16(__builtin_bit_cast(bf8, a), __builtin_bit_cast(bf8, b), c, 0, 0, 0); }
DI u32 pk(float lo, float hi) { bf2 r = __builtin_convertvector((f2){lo, hi}, bf2); return __builtin_bit_cast(u32, r); }
DI float blo(u32 v) { return __builtin_bit_cast(float, v << 16); }
DI float bhi(u32 v) { return __builtin_bit_cast(float, v & 0xffff0000u); }
template <class T> DI T ldsr(u32 a) { return *(LAS T*)(size_t)a; }
template <class T> DI void ldsw(u32 a, T v) { *(LAS T*)(size_t)a = v; }
DI v4u trfrag(u32 a) {
  const v2u lo = __builtin_bit_cast(v2u, __builtin_amdgcn_ds_read_tr16_b64_v4i16((LAS s4*)(size_t)a));
  const v2u hi = __builtin_bit_cast(v2u, __builtin_amdgcn_ds_read_tr16_b64_v4i16((LAS s4*)(size_t)(a + 512)));
  return (v4u){lo.x, lo.y, hi.x, hi.y};
}
DI f2 halves(float v) {
  auto r = __builtin_amdgcn_permlane32_swap(__builtin_bit_cast(u32, v), __builtin_bit_cast(u32, v), false, false);
  const u32 a = r[0], b = r[1]; return (f2){__builtin_bit_cast(float, a), __builtin_bit_cast(float, b)};
}
DI float bf(u32 v) { return __builtin_bit_cast(float, v); }
DI u32 fb(float v) { return __builtin_bit_cast(u32, v); }
DI float fadd(float a, float b) { float r; asm("v_add_f32 %0, %1, %2" : "=v"(r) : "v"(a), "v"(b)); return r; }
DI float hsum(float v) { const f2 r = halves(v); return r.x + r.y; }
DI float hmax(float v) { const f2 r = halves(v); return __builtin_fmaxf(r.x, r.y); }
DI float mx3(float a, float b, float c) { return __builtin_fmaxf(__builtin_fmaxf(a, b), c); }
DI float max16(const f16v& s, float m) {
  float a = mx3(m, s[0], s[1]), b = mx3(s[2], s[3], s[4]);
  a = mx3(a, s[5], s[6]); b = mx3(b, s[7], s[8]); a = mx3(a, s[9], s[10]); b = mx3(b, s[11], s[12]); a = mx3(a, s[13], s[14]);
  return mx3(a, b, s[15]);
}
DI v4u pk8(const f16v& v, int o) { return (v4u){pk(v[o], v[o + 1]), pk(v[o + 2], v[o + 3]), pk(v[o + 4], v[o + 5]), pk(v[o + 6], v[o + 7])}; }
DI bool alive(u64 t0) { return __builtin_amdgcn_s_memrealtime() - t0 < 200000000ull; }
DI u32 fresh(u32 v) { asm volatile("" : "+v"(v)); return v; }
DI u32 sfresh(u32 v) { asm volatile("" : "+s"(v)); return v; }
constexpr u32 EMPTY = 0xffffffffu;
DI u32 umax3(u32 a, u32 b, u32 c) { return max(max(a, b), c); }
DI u32 canon(u32 v) { return min(v, EMPTY - 1); }
DI v4u canon(v4u v) { return (v4u){canon(v.x), canon(v.y), canon(v.z), canon(v.w)}; }
DI bool empty2(const v4u& a, const v4u& b) { return __any(max(umax3(a.x, a.y, a.z), umax3(umax3(a.w, b.x, b.y), b.z, b.w)) == EMPTY); }

template <int P, int N, int NX> DI u32 validate1(v4u (&x)[NX]) {
  v4u &a = x[P % NX], &b = x[(P + 1) % NX];
  wait_vm<N>(a, b);
  u32 v = umax3(umax3(a.x, a.y, a.z), a.w, b.x); pin(v); return v;
}
template <int P, int NX> DI u64 validate2(u32 v, v4u (&x)[NX]) { const v4u& b = x[(P + 1) % NX]; u64 e = __ballot(max(umax3(v, b.y, b.z), b.w) == EMPTY); asm volatile("" : "+s"(e)); return e; }
template <int P, int NX, class FI> DI void validate3(u64 e, v4u (&x)[NX], FI&& issue, u64 t0) {
  v4u &a = x[P % NX], &b = x[(P + 1) % NX];
  if (__builtin_expect(e != 0, 0)) {
#pragma unroll 1
    do {
      __builtin_amdgcn_s_sleep(4);
      issue(ic<P>{}, a); issue(ic<P + 1>{}, b);
      wait_vm<0>(a, b);
    } while (empty2(a, b) && alive(t0));
  }
}
template <int N> DI void wait_lgkm() { __builtin_amdgcn_s_waitcnt(0xc07f | (N << 8)); }
constexpr int cmin(int a, int b) { return a < b ? a : b; }
constexpr int issued(int s, int KS, int LOOK, int INIT) { return s == 0 ? INIT : cmin(cmin(INIT + 2 * s, ((s - 1) & ~1) + LOOK), KS); }
constexpr int DEP = 5;
template <int NT, int KS, int KM> constexpr int ord_s(int i) { return i < KM * NT ? i / NT : KM + (i - KM * NT) % (KS > KM ? KS - KM : 1); }
template <int NT, int KS, int KM> constexpr int ord_t(int i) { return i < KM * NT ? i % NT : (i - KM * NT) / (KS > KM ? KS - KM : 1); }
template <int NT, int KS, int LOOK, int NX, bool SWAPLAST, bool VAL, int INIT = LOOK, int KM = KS, class FI, class FA, class FH>
DI void gemm(f16v* acc, v4u (&x)[NX], FI&& issue, FA&& addr, FH&& hook, u64 t0) {
  constexpr int N1 = KS * NT;
  v4u ring[DEP + 1];
  u32 vv; u64 ve;
  auto unit = [&](auto i) { return addr(ic<ord_s<NT, KS, KM>(i) * NT + ord_t<NT, KS, KM>(i)>{}); };
  sfor<DEP>([&](auto i) { ring[i] = trfrag(unit(i)); });
  SBAR();
  if constexpr (LOOK > 0 && VAL) { vv = validate1<0, INIT - 2>(x); ve = validate2<0>(vv, x); validate3<0>(ve, x, issue, t0); }
  if constexpr (LOOK > 0 && !VAL) wait_vm<INIT - 2>(x[0], x[1]);
  sfor<N1>([&](auto i_) {
    constexpr int i = i_, s = ord_s<NT, KS, KM>(i), t = ord_t<NT, KS, KM>(i);
    if constexpr ((i & 1) == 0 && i + DEP <= N1) { wait_lgkm<2 * DEP - 4>(); SBAR(); }
    if constexpr (SWAPLAST && t == NT - 1) acc[t] = mfma(x[s % NX], ring[i % (DEP + 1)], acc[t]);
    else acc[t] = mfma(ring[i % (DEP + 1)], x[s % NX], acc[t]);
    SBAR();
    hook(i_);
    SBAR();
    if constexpr (i + DEP < N1) ring[(i + DEP) % (DEP + 1)] = trfrag(unit(ic<i + DEP>{}));
    if constexpr (LOOK > 0 && i < KM * NT) {
      constexpr int f0 = issued(s, KS, LOOK, INIT), f1 = issued(s + 1, KS, LOOK, INIT);
      if constexpr (t < 2 && f0 + t < f1) { SBAR(); issue(ic<f0 + t>{}, x[(f0 + t) % NX]); }
      constexpr int G = KM == KS ? 2 * NT : NT, g = i % G, P = (i / G + 1) * 2;
      static_assert(KM == KS || 2 * KM == KS, "tail = half of the steps");
      if constexpr (P < KS) {
        if constexpr (VAL) {
          if constexpr (g == G - 3) { SBAR(); vv = validate1<P, f1 - (P + 2)>(x); }
          if constexpr (g == G - 2) { SBAR(); ve = validate2<P>(vv, x); }
          if constexpr (g == G - 1) { SBAR(); validate3<P>(ve, x, issue, t0); }
        } else if constexpr (g == G - 1) { SBAR(); wait_vm<f1 - (P + 2)>(x[P % NX], x[(P + 1) % NX]); }
      }
    }
    SBAR();
  });
}

#define STAMP() do {} while (0)
#define STAMPCLK(i) do {} while (0)
DI f2 ln_stats(float s1, float s2) {
  const float mean = s1 * (1.0f / 512.0f), var = __builtin_fmaxf(s2 * (1.0f / 512.0f) - mean * mean, 0.0f);
  return (f2){-mean, __builtin_amdgcn_sqrtf(var + 1e-5f)};
}
DI u32 trbase(u32 lane) { return (4 * (lane >> 5) + ((lane & 15) >> 2)) * 64 + ((lane >> 4) & 1) * 32 + (lane & 3) * 8; }
DI float ex2(float x) { return __builtin_amdgcn_exp2f(x); }
DI v4u expk8(const f16v& s, int o) { return (v4u){pk(ex2(s[o]), ex2(s[o + 1])), pk(ex2(s[o + 2]), ex2(s[o + 3])), pk(ex2(s[o + 4]), ex2(s[o + 5])), pk(ex2(s[o + 6]), ex2(s[o + 7]))}; }
DI v4u aux_tok(f2 st, u32 h) {
  const float v = h ? st.y : st.x; const u32 a = pk(v, v) & 0xffffu; const float r = v - blo(a); const u32 b = pk(r, r) & 0xffffu; const float r2 = r - blo(b);
  return (v4u){a | (b << 16), h ? pk(r2, 0.0f) : a, 0u, 0u};
}
DI v4u aux_col(u32 v) { return (v4u){(v & 0xffffu) | (v << 16), v >> 16, 0u, 0u}; }

struct Attn {
  v4u q[2], kf, v0, v1, p0, p1, t0, t1; f16v o[2], nm[2], sc[2]; u32 ka, va, vb, vstr, l16;
  DI void init(u32 lane, int qb) {
    l16 = lane * 16;
    const bool real = (lane & 16) == 0;
    ka = L_KV + qb * 4096 + l16; vstr = real ? 4096u : 512u;
    va = real ? L_KV + qb * 4096 + 2048 + l16 : L_ONES + qb * 512 + (lane & 15) * 16; vb = real ? va + 1024 : va;
  }
  DI f16v addmask() { f16v c; sfor<4>([&](auto g) { const f4 t = ldsr<f4>(L_TA + g * 1024 + l16); sfor<4>([&](auto i) { c[4 * g + i] = t[(int)i]; }); }); return c; }
  template <int D, int MASK, bool QK, bool KN, int PV> DI void stage(u32 a0, u32 a1, u32 kn) {
    const f16v& si = sc[D];
    float e[16];
    sfor<4>([&](auto i) { e[i] = ex2(si[(int)i]); });
    SBAR();
    wait_lgkm<0>();
    if constexpr (QK) sc[1 - D] = mfma(kf, q[1 - D], nm[1 - D]);
    SBAR();
    if constexpr (KN) kf = ldsr<v4u>(kn);
    sfor<4>([&](auto i) { e[4 + i] = ex2(si[4 + i]); });
    SBAR();
    if constexpr (PV == 1) o[1 - D] = mfma(v0, p0, o[1 - D]);
    if constexpr (PV == 2) o[1 - D] = mfma(v0, p0, (f16v){});
    SBAR();
    v0 = ldsr<v4u>(a0);
    sfor<4>([&](auto i) { e[8 + i] = ex2(si[8 + i]); });
    SBAR();
    if constexpr (PV) o[1 - D] = mfma(v1, p1, o[1 - D]);
    SBAR();
    v1 = ldsr<v4u>(a1);
    sfor<4>([&](auto i) { e[12 + i] = ex2(si[12 + i]); });
    SBAR();
    sfor<4>([&](auto i) { p0[(int)i] = pk(e[2 * i], e[2 * i + 1]); });
    sfor<4>([&](auto i) { p1[(int)i] = pk(e[8 + 2 * i], e[9 + 2 * i]); });
    if constexpr (MASK == 1) { pin(t0); pin(t1); p0 &= ~t0; p1 &= ~t1; }
    if constexpr (MASK == 2) { p0 &= t0; p1 &= t1; }
    pin(p0); pin(p1);
    SBAR();
  }
  DI void diag() {
    const f16v c = addmask();
    v4u kd[2];
    sfor<2>([&](auto d) { kd[d] = ldsr<v4u>(ka + 8 * 4096 + d * 1024); });
    t0 = ldsr<v4u>(L_TB + l16); t1 = ldsr<v4u>(L_TB + 1024 + l16);
    SBAR();
    sfor<2>([&](auto d) { sc[d] = mfma(kd[d], q[d], c); });
    va = fresh(va + 8 * vstr); vb = fresh(vb + 8 * vstr);
    SBAR();
    sfor<2>([&](auto d) {
      const float mx = hmax(max16(sc[d], sc[d][0]));
      sfor<16>([&](auto r) { nm[d][(int)r] = -mx; });
      pin(nm[d]);
      sc[d] = mfma(kd[d], q[d], nm[d]);
      SBAR();
    });
    stage<0, 1, false, false, 0>(va, vb, 0);
    stage<1, 1, false, false, 2>(va + 256, vb + 256, 0);
  }
  template <class F> DI void rest(F&& hint) {
    sc[0] = mfma(ldsr<v4u>(ka + 7 * 4096), q[0], nm[0]); kf = ldsr<v4u>(ka + 7 * 4096 + 1024);
    sfor<8>([&](auto i) {
      constexpr int j = 7 - i;
      va = fresh(va - vstr); vb = fresh(vb - vstr);
      if constexpr (j == 0) hint();
      stage<0, j == 0 ? 2 : 0, true, (j > 0), j == 7 ? 2 : 1>(va, vb, ka + (j - 1) * 4096);
      stage<1, j == 0 ? 2 : 0, (j > 0), (j > 0), 1>(va + 256, vb + 256, ka + (j - 1) * 4096 + 1024);
    });
    o[1] = mfma(v0, p0, o[1]); o[1] = mfma(v1, p1, o[1]);
  }
  template <int D> DI v4u finish(int jmin) {
    f16v& oo = o[D];
    float big = __builtin_fabsf(oo[0]); sfor<7>([&](auto r) { big += __builtin_fabsf(oo[r + 1]); });
    if (__builtin_expect(!__all(oo[8] <= 0x1p60f && big <= 0x1p126f), 0)) {
      auto scores = [&](int j) {
        f16v c = addmask();
        if (j != 8) sfor<16>([&](auto r) { c[(int)r] = (j == 0 && c[(int)r] == 0.0f) ? -__builtin_inff() : 0.0f; });
        return mfma(ldsr<v4u>(ka + j * 4096 + D * 1024), q[D], c);
      };
      float mx = -__builtin_inff();
#pragma unroll 1
      for (int j = jmin; j <= 8; ++j) mx = max16(scores(j), mx);
      mx = hmax(mx);
      oo = (f16v){};
#pragma unroll 1
      for (int j = jmin; j <= 8; ++j) {
        const f16v s = scores(j) - mx;
        oo = mfma(ldsr<v4u>(va + j * vstr + D * 256), expk8(s, 0), oo); oo = mfma(ldsr<v4u>(vb + j * vstr + D * 256), expk8(s, 8), oo);
      }
    }
    const float rl = __builtin_amdgcn_rcpf(oo[8]);
    sfor<8>([&](auto r) { oo[(int)r] *= rl; });
    return canon(pk8(oo, 0));
  }
};

constexpr int w2_kstep(int i) { return ((i % 14) / 2) * 8 + 2 * (i / 14) + (i & 1); }
constexpr int w2r_slot(int i) { return i < 6 ? 6 + 2 * i : 38 + 2 * (i - 6); }
constexpr int w2_pos(int i) { return i < 38 ? (i + 32) % 38 : (i - 6) % 39; }

__global__ void __launch_bounds__(512, 2) fwd(Params p) {
  extern __shared__ __attribute__((aligned(16))) unsigned char smem[];
  const u64 t0 = __builtin_amdgcn_s_memrealtime();
  u64 pc0; asm volatile("s_getpc_b64 %0" : "=s"(pc0));
  const int w0 = __builtin_amdgcn_readfirstlane(threadIdx.x >> 6);
  u32 dc = 0;
  if (w0 == 7) touch(dc, (threadIdx.x & 63) < 19 ? (threadIdx.x & 63) * 128 : 0x7fffffffu, mk_rsrc((const void*)pc0, CODE_KB * KB), (blockIdx.x >> 3) * 19 * 128);
#define IDS() const int w = sfresh(w0), bid = sfresh(blockIdx.x), grp = (bid & 6) * 2 + (bid >> 6), m = (bid & 1) * 8 + ((bid >> 3) & 7), cu = grp * 16 + m, hw = w & 3, cb = m & 7, th = m >> 3, \
              rt = w & 1, kh = (w >> 1) & 1, T13 = 4 * grp + hw, T24 = 4 * grp + 2 * th + rt, rot = (4 * (m & 1) + (grp & 3) + ((m >> 1) & 3)) & 7, rot2 = (2 * th + grp + cb) & 3, \
              rot4 = (cb + 2 * (grp & 3)) % 7, fs = m - th; const bool ffn = cb != 7; \
              (void)fs; (void)ffn; (void)rot; (void)rot2; (void)rot4; (void)cu; (void)cb; (void)kh; (void)T13; (void)T24
  const v4i rws = mk_rsrc(p.ws, WS_END);
  const u32 lane0 = threadIdx.x & 63;
  STAMP(); STAMPCLK(0);
  if (threadIdx.x < 4) ldsw<u32>(L_FLG + threadIdx.x * 4, 0u);
  BARRIER();
  v2u tagv;
  auto load_tags = [&]() { IDS(); const u32 g = grp + (lane0 < 3 ? lane0 : 0); bld8<0, true>(tagv, (g < 16 ? g : grp) * 256, rws, WS_EPOCH); };
  auto fix_tags = [&]() { pin(tagv); tagv = __builtin_bit_cast(v2u, __builtin_bit_cast(u64, tagv) + 1); };
  auto tag0 = [&]() { return (v2u){(u32)__builtin_amdgcn_readfirstlane(tagv.x), (u32)__builtin_amdgcn_readfirstlane(tagv.y)}; };
  auto publish = [&](int sid) { IDS(); const u32 lane = fresh(lane0); if (lane < 16) bst8(tag0(), lane * 128 + m * 8, rws, WS_FLAG + (sid * 256 + grp * 16) * 128); };
  auto await = [&](int sid, u32 first, u32 mask) {
    IDS(); const u64 tg = __builtin_bit_cast(u64, tag0()); const u32 vo = (fresh(lane0) & mask) * 8;
#pragma unroll 1
    for (;;) {
      v2u f; bld8<0, true>(f, vo, rws, WS_FLAG + (sid * 256 + cu) * 128 + first * 8); wait_vm<0>(f);
      if (__all(__builtin_bit_cast(u64, f) == tg) || !alive(t0)) break;
      __builtin_amdgcn_s_sleep(2);
    }
  };
  auto flg_wait = [&](bool q, u32 c) {
#pragma unroll 1
    for (u32 spins = 0; spins < (1u << 24); ++spins) { const v4u f = *(volatile LAS v4u*)(size_t)L_FLG; if ((u32)__builtin_amdgcn_readfirstlane(q ? f.x : min(f.y, f.z)) > c) break; if (w0 >= 4) __builtin_amdgcn_s_sleep(2); }
    asm volatile("" ::: "memory");
  };
  const u32 wrow = lane0 >> 2, wch = (lane0 & 3) * 16;
  if (w0 >= 4) {
    const v4i ro = mk_rsrc(p.wo, 512 * KB), r1 = mk_rsrc(p.w1, 1792 * KB), r2 = mk_rsrc(p.w2, 1792 * KB);
    u32 vo24, vo1;
    { IDS(); vo24 = wrow * 1024 + wch + cb * 128; vo1 = wrow * 3584 + wch + fs * 256; }
    auto stage_w1 = [&](int s0, int s1) {
      IDS();
      if (!ffn) return;
#pragma unroll 1
      for (int s = s0 + hw; s < s1; s += 4) {
        const u32 so = ((s + 17 + 4 * rot) & 31) * 16 * 3584;
        dma16(L_W1 + s * 4096, vo1, r1, so); dma16(L_W1 + s * 4096 + 1024, vo1, r1, so + 64); dma16(L_W1 + s * 4096 + 2048, vo1, r1, so + 128); dma16(L_W1 + s * 4096 + 3072, vo1, r1, so + 192);
      }
    };
    auto w2_row = [&](int i, int rot4) { const int t = (i * 37) >> 9, r = i - 14 * t, j = (r >> 1) + rot4; return (u32)(((j >= 7 ? j - 7 : j) * 8 + 2 * t + (r & 1)) * 16384); };
    auto stage_w2 = [&](int i0, int i1, int p0) {
      IDS();
#pragma unroll 1
      for (int i = i0 + hw; i < i1; i += 4) {
        const u32 la = (i + p0) * 4096, so = w2_row(i, rot4);
        dma16(la, vo24, r2, so); dma16(la + 1024, vo24, r2, so + 64); dma16(la + 2048, vo24, r2, so + 56 * 16384); dma16(la + 3072, vo24, r2, so + 56 * 16384 + 64);
      }
    };
    const v4u ones = {0x3f803f80u, 0x3f803f80u, 0x3f803f80u, 0x3f803f80u}, empt = {EMPTY, EMPTY, EMPTY, EMPTY};
    auto tables = [&]() {
      IDS(); const u32 lane = fresh(lane0), l16 = lane * 16; const int dq = (int)(lane & 31) - 4 * (int)(lane >> 5);
      sfor<4>([&](auto g) { f4 t; sfor<4>([&](auto i) { t[(int)i] = 8 * g + i <= dq ? 0.0f : -__builtin_inff(); }); ldsw<f4>(L_TA + g * 1024 + l16, t); });
      sfor<2>([&](auto g) { v4u b; sfor<4>([&](auto d) { const int c = 16 * g + 8 * (d >> 1) + 2 * (d & 1); b[(int)d] = (c > dq ? 0xffffu : 0u) | (c + 1 > dq ? 0xffff0000u : 0u); });
                            ldsw<v4u>(L_TB + g * 1024 + l16, b); });
      sfor<6>([&](auto g) { ldsw<v4u>(L_ONES + g * 1024 + l16, grp + g / 2 >= 2 ? ones : (v4u){}); });
    };
    auto cs_write = [&](const f16v& a, u32 dst) {
      const u32 lane = fresh(lane0);
      if ((lane & 31) == 0) sfor<4>([&](auto g) { v4u q; sfor<4>([&](auto i) { const float v = a[4 * g + i]; const u32 hi = pk(v, v) & 0xffffu; q[(int)i] = hi | (pk(0.0f, v - blo(hi)) & 0xffff0000u); });
                                                  ldsw<v4u>(dst + (8 * g + 4 * (lane >> 5)) * 4, q); });
    };
    auto put_stats = [&](f2 a, f2 b, u32 tok2) { ldsw<f4>(L_SD + tok2 * 16, (f4){a.x, a.y, b.x, b.y}); ldsw<f2>(L_RS + tok2 * 8, (f2){__builtin_amdgcn_rcpf(a.y), __builtin_amdgcn_rcpf(b.y)}); };
    v4u keep[24], w2r[30];
    {
      IDS();
      const u32 l16 = lane0 * 16;
      if (hw < 3) {
        const v4i rq = mk_rsrc(p.wqkv, 1536 * KB);
        const u32 vo = (8 * ((wrow >> 2) & 1) + 4 * (wrow >> 3) + (wrow & 3)) * 3072 + wch + m * 64 + hw * 1024;
        auto chunk = [&](int c) { const u32 so = 64 * ((c + rot) & 7) * 3072, la = L_WQ + c * 12288 + hw * 1024; sfor<4>([&](auto i) { dma16(la + i * 3072, vo, rq, so + i * 16 * 3072); }); };
        if (hw == 0) flg_wait(false, 2);
        load_tags();
        chunk(0);
        if (hw != 0) __builtin_amdgcn_s_sleep(40);
        chunk(1); chunk(2); chunk(3);
        f16v a = {}; const u32 b0 = L_WQ + hw * 1024 + trbase(lane0);
        auto landed = [&](int c) {
          __builtin_amdgcn_s_setprio(3);
          ldsw<u32>(L_FLG + hw * 4, c + 1);
          v4u q[4]; sfor<4>([&](auto i) { q[i] = trfrag(b0 + (4 * c + i) * 3072); }); SBAR();
          sfor<4>([&](auto i) { a = mfma(q[i], ones, a); });
        };
#pragma unroll 1
        for (int c = 0; c < 4; ++c) { WAIT_VM(12); ldsw<u32>(L_FLG + hw * 4, c + 1); __builtin_amdgcn_s_setprio(0); chunk(c + 4); landed(c); }
        WAIT_VM(12); landed(4); WAIT_VM(8); landed(5); WAIT_VM(4); landed(6); WAIT_VM(0); landed(7);
        cs_write(a, L_CSQ + hw * 128);
        asm volatile("s_waitcnt lgkmcnt(0)" ::: "memory");
        ldsw<u32>(L_FLG + hw * 4, 9);
        __builtin_amdgcn_s_setprio(0);
        fix_tags();
        if (hw == 1 && ffn) { const u32 v = ((const GAS u32*)p.b1)[fs * 64 + lane0]; ldsw<v2u>(L_B1 + lane0 * 8, (v2u){(v & 0xffffu) | (v << 16), (v >> 16) | (v & 0xffff0000u)}); }
      } else {
        u32 dd[4];
        touch(dd[0], lane0 < 48 ? lane0 * 128 : 0x7fffffffu, mk_rsrc(p.wqkv, 1536 * KB), bid * 6144);
        touch(dd[1], lane0 * 128, mk_rsrc(p.x, 2 * MB), (grp * 128 + m * 8) * 1024);
        load_tags();
        tables();
        WAIT_VM(0); fix_tags(); pin(dd[0]); pin(dd[1]); pin(dc);
        flg_wait(false, 5);
        touch(dd[0], lane0 < 16 ? lane0 * 256 * 128 : 0x7fffffffu, rws, WS_FLAG + cu * 128);
        touch(dd[1], lane0 < 56 ? lane0 * 128 : 0x7fffffffu, r1, bid * 7168); touch(dd[3], lane0 < 16 ? lane0 * 128 : 0x7fffffffu, ro, bid * 2048); touch(dd[2], lane0 < 56 ? lane0 * 128 : 0x7fffffffu, r2, bid * 7168);
        WAIT_VM(0); pin(dd[0]); pin(dd[1]); pin(dd[2]); pin(dd[3]);
      }
      if (STAMPL) { STAMP(); STAMP(); }
    }
    sfor<2>([&](auto L_) {
      constexpr int L = L_;
      IDS();
      const u32 lane = fresh(lane0), l16 = lane * 16;
      auto gather_stats = [&](int buf) {
        __builtin_amdgcn_s_setprio(3);
        v4u q[4]; const u32 so = WS_ST + buf * 256 * KB + T13 * 4096;
#pragma unroll 1
        for (;;) {
          bld16<0, true>(q[0], l16, rws, so); bld16<1024, true>(q[1], l16, rws, so); bld16<2048, true>(q[2], l16, rws, so); bld16<3072, true>(q[3], l16, rws, so);
          wait_vm<0>(q[0], q[1]); wait_vm<0>(q[2], q[3]);
          if ((!empty2(q[0], q[1]) && !empty2(q[2], q[3])) || !alive(t0)) break;
          __builtin_amdgcn_s_sleep(4);
        }
        float s[4];
        sfor<4>([&](auto i) { const u32 a = q[0][(int)i], b = q[1][(int)i], c = q[2][(int)i], d = q[3][(int)i];
                              float v = fadd(fadd(bf(a), bf(b)), fadd(bf(c), bf(d)));
                              auto r = __builtin_amdgcn_permlane16_swap(fb(v), fb(v), false, false); const u32 r0 = r[0], r1 = r[1];
                              const f2 hh = halves(fadd(bf(r0), bf(r1))); s[i] = fadd(hh.x, hh.y); });
        if (lane < 16) put_stats(ln_stats(s[0], s[1]), ln_stats(s[2], s[3]), hw * 16 + lane);
        __builtin_amdgcn_s_setprio(0);
      };
      auto fill_x = [&](int buf) { const u32 so = WS_XF + buf * 2 * MB + (T24 * 32 + cb * 4 + kh * 2) * 1024; bst16<0>(empt, l16, rws, so); bst16<1024>(empt, l16, rws, so);
                                   if (lane < 16) bst16<0>(empt, l16, rws, WS_ST + buf * 256 * KB + (T24 * 16 + cb * 2 + kh) * 256); };
      auto fill_h = [&](auto i0, auto n) { if (ffn) sfor<n>([&](auto j) { constexpr int i = i0 + j; bst16<(i & 3) * 1024>(empt, l16, rws, WS_HF + L * 7 * MB + (T13 * 112 + fs * 8 + (i & 4)) * 1024); }); };
      auto stage_wo = [&](int s0, int s1) {
#pragma unroll 1
        for (int s = s0 + hw; s < s1; s += 4) { const u32 so = ((s & 16) + ((s + 4 * rot2) & 15)) * 16384; dma16(L_WO + s * 2048, vo24, ro, so); dma16(L_WO + s * 2048 + 1024, vo24, ro, so + 64); } };
      auto w2_get = [&](v4u* r, int s0) { const u32 so = w2_row(s0, rot4) + (hw & 1) * 16384 + (hw >> 1) * 56 * 16384; bld16<0, false>(r[0], vo24, r2, so); bld16<64, false>(r[1], vo24, r2, so); };
      auto w2_set = [&](v4u* r, int s0, u32 ta) { pin(r[0]); pin(r[1]); const u32 o = ta + ((hw & 1) ? w2_pos(s0 + 1) : w2_pos(s0)) * 4096; ldsw<v4u>(o, r[0]); ldsw<v4u>(o + 1024, r[1]); };
      auto w2_regs = [&](auto i0, auto n) { sfor<n>([&](auto j) { constexpr int i = i0 + j; w2_get(&w2r[2 * i], w2r_slot(i)); }); };
      auto w2_put = [&](auto i0, auto n) { const u32 ta = fresh(l16 + (hw >> 1) * 2048); sfor<n>([&](auto j) { constexpr int i = i0 + j; w2_set(&w2r[2 * i], w2r_slot(i), ta); }); };
      v4u st1[24];
      auto w1_pos = [&](int i) { const int j = hw + 4 * i; return j < 11 ? j : j == 11 ? 23 : j < 16 ? j - 1 : j + 8; };
      auto w1_get = [&](auto i0, auto n) { if (ffn) sfor<n>([&](auto j) { constexpr int i = i0 + j; const u32 so = ((w1_pos(i) + 17 + 4 * rot) & 31) * 16 * 3584;
                                                                             sfor<4>([&](auto t) { bld16<t * 64, false>(st1[4 * i + t], vo1, r1, so); }); }); };
      auto w1_put = [&](auto i0, auto n) { if (ffn) sfor<n>([&](auto j) { constexpr int i = i0 + j; const u32 o = fresh(l16) + L_W1 + w1_pos(i) * 4096;
                                                                             sfor<4>([&](auto t) { pin(st1[4 * i + t]); ldsw<v4u>(o + t * 1024, st1[4 * i + t]); }); }); };
      f16v cs = {};
      auto w1_colsum = [&](int p0, int p1) {
        const u32 b0 = L_W1 + hw * 1024 + trbase(lane);
#pragma unroll 1
        for (int z = p0; z < p1; z += 4) { v4u q[4]; sfor<4>([&](auto i) { q[i] = trfrag(b0 + (z + i < p1 ? z + i : p0) * 4096); }); SBAR();
                                           sfor<4>([&](auto i) { cs = mfma(q[i], z + i < p1 ? ones : (v4u){}, cs); }); }
      };
      auto halo = [&]() {
        __builtin_amdgcn_s_setprio(3);
        if (4 * grp + 2 * hw >= 8) {
          await(L, 2 * hw, 1);
          sfor<8>([&](auto i) { dma16<true>(L_KV + hw * 8192 + i * 1024, l16, rws, WS_KV + L * 4 * MB + ((4 * grp - 8 + 2 * hw + (i >> 2)) * 16 + m) * 4096 + (i & 3) * 1024); });
        } else sfor<8>([&](auto i) { ldsw<v4u>(L_KV + hw * 8192 + i * 1024 + l16, (v4u){}); });
        __builtin_amdgcn_s_setprio(0);
      };
      auto hint = [&](int sid) { if (hw == 3) { __builtin_amdgcn_s_setprio(3); publish(sid); __builtin_amdgcn_s_setprio(0); } };
      v2u hf;
      auto ask = [&](int sid) { if (hw == 2) bld8<0, true>(hf, (lane & 15) * 8, rws, WS_FLAG + (sid * 256 + cu) * 128); };
      auto hinted = [&](int sid) { if (hw == 2) { __builtin_amdgcn_s_setprio(3); wait_vm<0>(hf); if (!__all(__builtin_bit_cast(u64, hf) == __builtin_bit_cast(u64, tag0()))) await(sid, 0, 15); __builtin_amdgcn_s_setprio(0); } };
      if constexpr (L == 1) HBAR();
      if (L == 1 && hw == 3) tables();
      if (L == 1 || hw != 0) w1_get(ic<3>{}, ic<3>{});
      HBAR();
      fill_h(ic<0>{}, ic<2>{});
      if (L == 1) __builtin_amdgcn_s_sleep(6);
      else sfor<24>([&](auto i) { keep[i] = ldsr<v4u>(L_WQ + hw * 24 * KB + i * 1024 + l16); });
      halo();
      fill_x(2 * L);
      WAIT_VM(3); BARRIER();
      { const u32 so = WS_OF + L * 2 * MB + (T13 * 32 + 2 * m) * 1024; bst16<0>(empt, l16, rws, so); bst16<1024>(empt, l16, rws, so); }
      stage_wo(0, 32);
      stage_w1(15, 23);
      w1_get(ic<0>{}, ic<3>{});
      if (ffn) WAIT_VM(12); else WAIT_VM(0);
      HBAR();
      hint(3 + L);
      if (L == 0 && hw == 0) w1_get(ic<3>{}, ic<3>{});
      fill_h(ic<2>{}, ic<2>{});
      if (L == 0) { w2_regs(ic<0>{}, ic<3>{}); WAIT_VM(8); } else WAIT_VM(2);
      BARRIER();
      ask(3 + L);
      w1_put(ic<0>{}, ic<2>{});
      if (hw < 3) w1_put(ic<2>{}, ic<1>{});
      hinted(3 + L);
      BARRIER();
      if (L == 0 && ffn) w1_colsum(0, 11);
      HBAR();
      hint(5 + L);
      fill_h(ic<4>{}, ic<2>{});
      if (L == 0 && ffn) w1_colsum(15, 19);
      BARRIER();
      ask(5 + L);
      w1_put(ic<4>{}, ic<2>{});
      if (hw == 3) w1_put(ic<2>{}, ic<1>{});
      hinted(5 + L);
      stage_w2(0, 6, 32);
      if (L == 0) w2_regs(ic<3>{}, ic<1>{});
      BARRIER();
      gather_stats(2 * L);
      fill_h(ic<6>{}, ic<2>{});
      w1_put(ic<3>{}, ic<1>{});
      if (L == 0 && ffn) w1_colsum(19, 32);
      WAIT_VM(0);
      HBAR();
      hint(8 + L);
      if (L == 0 && ffn) { __builtin_amdgcn_s_setprio(3); w1_colsum(11, 15); cs_write(cs, L_CS1 + hw * 128); __builtin_amdgcn_s_setprio(0); }
      HBAR();
      if (L == 0) w2_regs(ic<4>{}, ic<2>{});
      stage_w2(21, 37, -6);
      ask(8 + L); hinted(8 + L);
      if (L == 0) { w2_regs(ic<6>{}, ic<9>{}); WAIT_VM(34); }
      BARRIER();
      stage_w2(hw < 3 ? 18 : 34, hw < 3 ? 21 : 38, -6);
      w2_put(ic<0>{}, ic<6>{});
      BARRIER();
      WAIT_VM(0);
      HBAR();
      HBAR();
      w2_put(ic<6>{}, ic<9>{});
      HBAR();
      if (L == 0) fill_x(1);
      HBAR();
      const u32 ka = fresh(L_WQ + hw * 24 * KB + (8 * ((lane >> 4) & 1) + 4 * (lane >> 5) + ((lane >> 2) & 3)) * 64 + (lane & 3) * 16);
      if (L == 0 && hw < 2) sfor<12>([&](auto i) { ldsw<v4u>(ka + i * 1024, keep[i]); });
      WAIT_VM(0);
      HBAR();
      if (L == 0) hint(7);
      if (L == 0 && hw < 2) sfor<12>([&](auto i) { ldsw<v4u>(ka + (12 + i) * 1024, keep[12 + i]); });
      BARRIER();
      if constexpr (L == 0) {
        ask(7);
        if (hw >= 2) sfor<24>([&](auto i) { ldsw<v4u>(ka + i * 1024, keep[i]); });
        hinted(7);
        BARRIER();
        gather_stats(1);
      }
    });
    STAMP();
    return;
  }
  v4u res[2], b2p[2];
  v4u qf[2];
  struct Aux { v4u tf; u32 c[4]; float rs; f2 st; f4 rv[4]; };
  auto aux_read = [&](Aux& x, bool w1) {
    IDS();
    const u32 lane = fresh(lane0), h = lane >> 5, t32 = lane & 31;
    x.st = ldsr<f2>(L_SD + hw * 256 + t32 * 8);
    x.rs = ldsr<float>(L_RS + hw * 128 + t32 * 4);
    if (w1) { const u32 lc = fresh(L_CS1 + h * 512 + t32 * 4); sfor<4>([&](auto t) { x.c[t] = ldsr<u32>(lc + t * 128); }); }
    else { const u32 lc = fresh(L_CSQ + t32 * 4), lr = fresh(L_RS + hw * 128 + 16 * h);
           sfor<3>([&](auto t) { x.c[t] = ldsr<u32>(lc + t * 128); });
           sfor<4>([&](auto g) { x.rv[g] = ldsr<f4>(lr + 32 * g); }); }
  };
  auto aux_make = [&](Aux& x, bool w1) { const u32 h = fresh(lane0) >> 5; x.tf = aux_tok(x.st, h); if (!w1 && h) x.tf = (v4u){}; pin(x.tf); };
  v4u kv[4]; u32 lk;
  auto kv_epilogue = [&](u32 so, const Aux& x, f16v& ak, f16v& av) {
    const u32 l16 = fresh(lane0) * 16;
    ak *= x.rs;
    kv[0] = pk8(ak, 0); kv[1] = pk8(ak, 8); bst16<0>(kv[0], l16, rws, so); bst16<1024>(kv[1], l16, rws, so);
    SBAR();
    sfor<16>([&](auto r) { av[(int)r] *= x.rv[r / 4][r % 4]; });
    kv[2] = pk8(av, 0); kv[3] = pk8(av, 8); bst16<2048>(kv[2], l16, rws, so); bst16<3072>(kv[3], l16, rws, so);
    lk = fresh(L_KV + (8 + w0) * 4096 + l16);
  };
  auto q_epilogue = [&](const Aux& x, f16v aq) { aq *= x.rs * (0.25f * 1.4426950408889634f); qf[0] = pk8(aq, 0); qf[1] = pk8(aq, 8); };
  u32 kvf; { IDS(); kvf = (lane0 == 1 || lane0 == 2) && grp + lane0 < 16 ? WS_FLAG + cu * 128 + lane0 * (16 * 128 - 32) + 64 + hw * 8 : 0x7fffffffu; }
  auto kv_flag = [&](int L) { WAIT_VM(0); bst8(tagv, kvf, rws, L * 256 * 128); };
  {
    IDS();
    const u32 lane = lane0, h = lane >> 5, t32 = lane & 31;
    const v4i rx = mk_rsrc(p.x, 2 * MB);
    const u32 vo = wrow * 1024 + (((lane & 3) ^ ((lane >> 4) & 3)) * 16), so = T13 * 32 * 1024, xs = hw * 12 * KB;
    auto issue = [&](int u) { const u32 o = u < 16 ? so + ((u + 2 * rot) & 15) * 64 : T24 * 32 * 1024 + cb * 128 + kh * 64;
                              dma16(xs + (u % 6) * 2048, vo, rx, o); dma16(xs + (u % 6) * 2048 + 1024, vo, rx, o + 16384); };
    sfor<6>([&](auto u) { issue(u); });
    const u32 xa0 = xs + t32 * 64 + ((h ^ ((t32 >> 2) & 3)) * 16), xa1 = xa0 ^ 32;
    v4u xq[32]; v2u bb[4];
    float s1 = 0.f, s2 = 0.f;
    sfor<17>([&](auto u) {
      if constexpr (u < 16) {
        asm volatile("s_waitcnt vmcnt(%0)" ::"i"(2 * ((u == 0 ? 5 : u + 4 < 16 ? u + 4 : 16) - u) + (u > 12 ? 5 : 0)) : "memory");
        xq[2 * u] = ldsr<v4u>(xa0 + (u % 6) * 2048); xq[2 * u + 1] = ldsr<v4u>(xa1 + (u % 6) * 2048);
        SBAR();
      }
      if constexpr (u > 0) {
        pin(xq[2 * u - 2]); pin(xq[2 * u - 1]);
        if constexpr (u + 5 < 17) issue(u + 5);
        if constexpr (u == 12) { load_tags(); const v4i rb = mk_rsrc(p.b2, 1024); const u32 bo = (cb * 64 + kh * 32 + 4 * h) * 2; sfor<4>([&](auto i) { bld8<i * 16, false>(bb[i], bo, rb, 0); }); }
        sfor<8>([&](auto i) { const u32 eu = xq[2 * u - 2 + i / 4][i % 4]; const bf2 e = __builtin_bit_cast(bf2, eu);
                              s1 = __builtin_amdgcn_fdot2_f32_bf16(e, __builtin_bit_cast(bf2, 0x3f803f80u), s1, false); s2 = __builtin_amdgcn_fdot2_f32_bf16(e, e, s2, false); });
        pin(s1); pin(s2);
      }
      SBAR();
    });
    { const f2 st = ln_stats(hsum(s1), hsum(s2)); ldsw<f2>(L_SD + hw * 256 + t32 * 8, st); ldsw<float>(L_RS + hw * 128 + t32 * 4, __builtin_amdgcn_rcpf(st.y)); }
    WAIT_VM(0);
    fix_tags();
    sfor<4>([&](auto i) { pin(bb[i]); });
    sfor<2>([&](auto f) { const u32 ra = xs + (16 % 6) * 2048 + t32 * 64 + 8 * h, z = ((t32 >> 2) & 3) * 16;
                          const v2u a = ldsr<v2u>(ra + ((2 * f * 16) ^ z)), b = ldsr<v2u>(ra + (((2 * f + 1) * 16) ^ z));
                          res[f] = (v4u){a.x, a.y, b.x, b.y}; b2p[f] = (v4u){bb[2 * f].x, bb[2 * f].y, bb[2 * f + 1].x, bb[2 * f + 1].y}; pin(res[f]); });
    const u32 wb0 = fresh(L_WQ + trbase(lane)), wb1 = fresh(wb0 + 48 * KB);
    auto addr = [&](auto i) { constexpr int o = (i / 2) * 3072 + 1024 + (i & 1) * 1024; return o < 48 * KB ? wb0 + o : wb1 + (o - 48 * KB); };
    auto addrq = [&](auto i) { constexpr int o = i * 3072; return o < 48 * KB ? wb0 + o : wb1 + (o - 48 * KB); };
    f16v acc1[2] = {};
    if (STAMPL) STAMP();
    flg_wait(false, 1);
    gemm<2, 32, 0, 32, true, false>(acc1, xq, 0, addr, [&](auto i) { constexpr int n = i + DEP; if constexpr (n == 16 || n == 32 || n == 48 || n == 56) { pin(acc1[0]); pin(acc1[1]); flg_wait(false, n / 8 + (n < 48)); } }, t0);
    pin(acc1[0]); pin(acc1[1]);
    flg_wait(false, 8);
    if (STAMPL) STAMP();
    Aux ax; aux_read(ax, false);
    SBAR();
    aux_make(ax, false);
    acc1[0] = mfma(aux_col(ax.c[1]), ax.tf, acc1[0]); acc1[1] = mfma(ax.tf, aux_col(ax.c[2]), acc1[1]);
    kv_epilogue(WS_KV + (T13 * 16 + m) * 4096, ax, acc1[0], acc1[1]);
    flg_wait(true, 7);
    f16v accq = {};
    gemm<1, 32, 0, 32, false, false>(&accq, xq, 0, addrq, [&](auto i) {
      if constexpr (i == 14) { kv_flag(0); LBAR(); ax.c[0] = ldsr<u32>(L_CSQ + t32 * 4); }
      if constexpr (i % 4 == 0 && i >= 16) ldsw<v4u>(lk + (i / 4 - 4) * 1024, kv[i / 4 - 4]);
      if constexpr (i == 22) accq = mfma(aux_col(ax.c[0]), ax.tf, accq); }, t0);
    q_epilogue(ax, accq);
  }
  for (int L = 0;; ++L) {
    IDS();
    const u32 lane = fresh(lane0), l16 = lane * 16, h = lane >> 5, t32 = lane & 31;
    auto res_init = [&](f16v (&acc)[2], bool bias) {
      sfor<8>([&](auto i) { const u32 r = res[i / 4][i % 4], b = bias ? fresh(b2p[i / 4][i % 4]) : 0u;
                            acc[0][2 * i] = bias ? blo(r) + blo(b) : blo(r); acc[0][2 * i + 1] = bias ? bhi(r) + bhi(b) : bhi(r); });
      acc[1] = (f16v){};
      pin(acc[0]);
    };
    auto res_epilogue = [&](f16v (&acc)[2], int buf, bool last) {
      const u32 xa = fresh(L_XCH + w * 4096 + l16), xb = fresh(L_XCH + (w ^ 2) * 4096 + l16);
      const u32 so = sfresh(WS_XF + buf * 2 * MB + (T24 * 32 + cb * 4 + kh * 2) * 1024), ss = sfresh(WS_ST + buf * 256 * KB + (T24 * 16 + cb * 2 + kh) * 256);
      sfor<4>([&](auto g) { ldsw<f4>(xa + g * 1024, (f4){acc[1][4 * g], acc[1][4 * g + 1], acc[1][4 * g + 2], acc[1][4 * g + 3]}); });
      BARRIER();
      f4 o[4]; sfor<4>([&](auto g) { o[g] = ldsr<f4>(xb + g * 1024); });
      f16v v; sfor<16>([&](auto r) { v[(int)r] = acc[0][(int)r] + o[r / 4][r % 4]; });
      res[0] = canon(pk8(v, 0)); res[1] = canon(pk8(v, 8));
      if (!last) {
        bst16<0>(res[0], l16, rws, so); bst16<1024>(res[1], l16, rws, so);
        SBAR();
        float s1 = 0.f, s2 = 0.f;
        sfor<8>([&](auto i) { const u32 eu = res[i / 4][i % 4]; const bf2 e = __builtin_bit_cast(bf2, eu);
                              s1 = __builtin_amdgcn_fdot2_f32_bf16(e, __builtin_bit_cast(bf2, 0x3f803f80u), s1, false); s2 = __builtin_amdgcn_fdot2_f32_bf16(e, e, s2, false); });
        s1 = hsum(s1); s2 = hsum(s2);
        bst8((v2u){canon(fb(s1)), canon(fb(s2))}, t32 * 8, rws, ss);
        WAIT_VM(1);
        BARRIER();
      } else {
        GAS unsigned char* op = (GAS unsigned char*)p.out + (T24 * 32 + t32) * 1024 + (cb * 64 + kh * 32 + 8 * h) * 2;
        sfor<2>([&](auto f) {
          auto a = __builtin_amdgcn_permlane32_swap(res[f].x, res[f].z, false, false), b = __builtin_amdgcn_permlane32_swap(res[f].y, res[f].w, false, false);
          *(GAS v4u*)(op + f * 32) = (v4u){a[0], b[0], a[1], b[1]}; });
      }
    };
    {
      Attn at; at.init(lane, hw); at.q[0] = qf[0]; at.q[1] = qf[1];
      at.diag();
      BARRIER();
      at.rest([&]() { LBAR();   });
      const u32 so = WS_OF + L * 2 * MB + (T13 * 32 + 2 * m) * 1024; const int jmin = max(8 - T13, 0);
      bst16<0>(at.finish<0>(jmin), l16, rws, so); bst16<1024>(at.finish<1>(jmin), l16, rws, so);
    }
    BARRIER();
    WAIT_VM(0);
    BARRIER();
    {
      constexpr int LOOK = 10;
      v4u win[LOOK];
      f16v acc2[2];
      const u32 so = WS_OF + L * 2 * MB + (T24 * 32 + kh * 16) * 1024, wb = fresh(L_WO + kh * 33 * KB + trbase(lane)), wc = fresh(wb ^ 1024);
      auto issue = [&](auto i, v4u& d) { bld16<(i & 3) * 1024, true>(d, l16, rws, so + (((i >> 2) + rot2) & 3) * 4096); };
      auto addr = [&](auto i) { return ((i & 1) ? wc : wb) + (i / 2) * 2048; };
      sfor<10>([&](auto i) { issue(i, win[i]); });
      res_init(acc2, false);
      gemm<2, 16, LOOK, LOOK, false, true, 10>(acc2, win, issue, addr, [&](auto i) { if constexpr (i == 16) LBAR();   }, t0);
      res_epilogue(acc2, 2 * L, false);
    }
    const u32 soh = WS_HF + L * 7 * MB + (T24 * 112 + kh * 56) * 1024;
    constexpr int LOOK4 = 12;
    v4u hw4[LOOK4];
    u32 pb[7];
    auto h_bases = [&]() { sfor<7>([&](auto j) { const int jj = j + rot4; pb[j] = soh + (jj >= 7 ? jj - 7 : jj) * 8192; }); };
    if (ffn) {
      constexpr int LOOK = 22;
      v4u win[LOOK];
      f16v acc3[4] = {};
      const u32 so = WS_XF + 2 * L * 2 * MB + T13 * 32 * 1024, wb0 = fresh(L_W1 + trbase(lane)), wb1 = fresh(wb0 + 63 * KB), wb2 = fresh(wb0 + 126 * KB);
      const u32 sh = WS_HF + L * 7 * MB + (T13 * 112 + fs * 8) * 1024;
      auto issue = [&](auto i, v4u& d) { bld16<(i & 3) * 1024, true>(d, l16, rws, so + (((i >> 2) + rot) & 7) * 4096); };
      auto addr = [&](auto i) { constexpr int o = ((i + 60) & 127) * 1024; return o < 63 * KB ? wb0 + o : o < 126 * KB ? wb1 + (o - 63 * KB) : wb2 + (o - 126 * KB); };
      sfor<8>([&](auto i) { issue(i, win[i]); });
      Aux ax;
      constexpr float K1 = -2.0f * 0.7978845608028654f * 1.4426950408889634f, K2 = K1 * 0.044715f;
      float c1, c2, za, zb; v4u q;
      auto gelu_stage = [&](auto e, auto g) {
        const float ua = acc3[e / 8][2 * (e % 8)], ub = acc3[e / 8][2 * (e % 8) + 1], sd = ax.st.y;
        if constexpr (g == 0) { za = __builtin_fmaf(ua * ua, c2, c1); zb = __builtin_fmaf(ub * ub, c2, c1); }
        if constexpr (g == 1) { za = ex2(za * ua); zb = ex2(zb * ub); }
        if constexpr (g == 2) { za = __builtin_amdgcn_rcpf(__builtin_fmaf(za, sd, sd)); zb = __builtin_amdgcn_rcpf(__builtin_fmaf(zb, sd, sd)); }
        if constexpr (g == 3) { za *= ua; zb *= ub; }
        pin(za); pin(zb);
        if constexpr (g == 3) { u32 r = canon(pk(za, zb)); pin(r); q[e % 4] = r; if constexpr (e % 4 == 3) bst16<((e / 4) & 3) * 1024>(q, l16, rws, sh); }
      };
      gemm<4, 32, LOOK, LOOK, false, true, 8, 16>(acc3, win, issue, addr, [&](auto i) {
        if constexpr (i == 40) LBAR();
        if constexpr (i == 63) { LBAR(); aux_read(ax, true); }
        if constexpr (i == 68) { aux_make(ax, true); c1 = K1 * ax.rs; c2 = K2 * ax.rs * ax.rs * ax.rs; pin(c1); pin(c2); }
        if constexpr (i >= 72 && i < 76) acc3[i & 3] = mfma(aux_col(ax.c[i & 3]), ax.tf, acc3[i & 3]);
        if constexpr (i >= 80) gelu_stage(ic<(i - 80) / 4>{}, ic<i % 4>{});
      }, t0);
      BARRIER();
      sfor<5>([&](auto f_) {
        constexpr int f = 3 + f_;
        sfor<4>([&](auto d) {
          const f2 u = {acc3[f / 2][(f & 1) * 8 + 2 * d], acc3[f / 2][(f & 1) * 8 + 2 * d + 1]}, sd = {ax.st.y, ax.st.y};
          const f2 z = u * __builtin_elementwise_fma(u * u, (f2){c2, c2}, (f2){c1, c1});
          const f2 dn = __builtin_elementwise_fma((f2){ex2(z.x), ex2(z.y)}, sd, sd);
          const f2 g = u * (f2){__builtin_amdgcn_rcpf(dn.x), __builtin_amdgcn_rcpf(dn.y)};
          q[(int)d] = canon(pk(g.x, g.y));
        });
        bst16<(f & 3) * 1024>(q, l16, rws, sh + (f >> 2) * 4096);
        SBAR();
        if constexpr (f == 3) h_bases();
        if constexpr (f >= 4) {
          sfor<LOOK4 / 4>([&](auto j) { constexpr int i = LOOK4 / 4 * (f - 4) + j, k = w2_kstep(i); bld16<(k & 3) * 1024, true>(hw4[i], l16, rws, pb[k >> 3]); });
          SBAR();
        }
      });
    } else {
      LBAR(); LBAR(); BARRIER();
      h_bases();
#pragma unroll 1
      for (;;) {
        __builtin_amdgcn_s_sleep(8);
        bld16<2048, true>(hw4[0], l16, rws, soh); bld16<3072, true>(hw4[1], l16, rws, soh); wait_vm<0>(hw4[0], hw4[1]);
        if (!empty2(hw4[0], hw4[1]) || !alive(t0)) break;
      }
      sfor<LOOK4>([&](auto i) { constexpr int k = w2_kstep(i); bld16<(k & 3) * 1024, true>(hw4[i], l16, rws, pb[k >> 3]); });
    }
    {
      f16v acc2[2];
      const u32 wb0 = fresh(kh * 3072 + trbase(lane)), wb1 = fresh(wb0 + 60 * KB), wb2 = fresh(wb0 + 120 * KB), wc0 = fresh(wb0 ^ 1024), wc1 = fresh(wb1 ^ 1024), wc2 = fresh(wb2 ^ 1024);
      const u32 l16b = l16 + 4096;
      auto issue = [&](auto i, v4u& d) { constexpr int k = w2_kstep(i); bld16<(k & 3) * 1024, true>(d, (k & 4) ? l16b : l16, rws, pb[k >> 3]); };
      auto addr = [&](auto i) { constexpr int o = w2_pos(i / 2) * 4096; constexpr bool c = i & 1;
                                return o < 60 * KB ? (c ? wc0 : wb0) + o : o < 120 * KB ? (c ? wc1 : wb1) + (o - 60 * KB) : (c ? wc2 : wb2) + (o - 120 * KB); };
      res_init(acc2, true);
      BARRIER();
      gemm<2, 56, LOOK4, LOOK4, false, true>(acc2, hw4, issue, addr, [&](auto i) { if constexpr (i == 36 - DEP || i == 33 || i == 76 - DEP || i == 75 || i == 96) LBAR();   }, t0);
      res_epilogue(acc2, 1, L == 1);
    }
    if (L == 1) break;
    {
      constexpr int LOOK = 12;
      const u32 so = WS_XF + 1 * 2 * MB + T13 * 32 * 1024, wb0 = fresh(L_WQ + trbase(lane)), wb1 = fresh(wb0 + 48 * KB);
      auto issue = [&](auto i, v4u& d) { bld16<(i & 3) * 1024, true>(d, l16, rws, so + (((i >> 2) + rot) & 7) * 4096); };
      auto addr = [&](auto i) { constexpr int o = (i / 2) * 3072 + 1024 + (i & 1) * 1024; return o < 48 * KB ? wb0 + o : wb1 + (o - 48 * KB); };
      auto addrq = [&](auto i) { constexpr int o = i * 3072; return o < 48 * KB ? wb0 + o : wb1 + (o - 48 * KB); };
      const u32 sk = sfresh(WS_KV + 4 * MB + (T13 * 16 + m) * 4096);
      f16v acc1[2] = {};
      v4u xq[32];
      sfor<12>([&](auto i) { issue(i, xq[i]); });
      Aux ax;
      gemm<2, 32, LOOK, 32, true, true, 12>(acc1, xq, issue, addr, [&](auto i) {
        if constexpr (i == 40) { LBAR(); aux_read(ax, false); }
        if constexpr (i == 48) aux_make(ax, false);
        if constexpr (i == 52) acc1[0] = mfma(aux_col(ax.c[1]), ax.tf, acc1[0]);
        if constexpr (i == 53) acc1[1] = mfma(ax.tf, aux_col(ax.c[2]), acc1[1]);
      }, t0);
      kv_epilogue(sk, ax, acc1[0], acc1[1]);
      f16v accq = mfma(aux_col(ax.c[0]), ax.tf, (f16v){});
      gemm<1, 32, 0, 32, false, false>(&accq, xq, 0, addrq, [&](auto i) {
        if constexpr (i % 4 == 1 && i < 16) ldsw<v4u>(lk + (i / 4) * 1024, kv[i / 4]);
        if constexpr (i == 17) { kv_flag(1); LBAR();   } }, t0);
      q_epilogue(ax, accq);
    }
  }
  STAMP(); STAMPCLK(1);
  { IDS(); if (w == 0 && m == 0 && lane0 == 0) bst8(tagv, 0, rws, WS_EPOCH + grp * 256); }
}

extern "C" void kernel_launch(void* const* d_in, const int* in_sizes, int n_in, void* d_out, int out_size, void* d_ws, size_t ws_size, hipStream_t stream) {
  Params p;
  p.x = d_in[0]; p.wqkv = d_in[1]; p.wo = d_in[2]; p.w1 = d_in[4]; p.b1 = d_in[5]; p.w2 = d_in[6]; p.b2 = d_in[7]; p.out = d_out; p.ws = (unsigned char*)d_ws;
  (void)hipFuncSetAttribute((const void*)fwd, hipFuncAttributeMaxDynamicSharedMemorySize, LDS_BYTES);
  hipLaunchKernelGGL(fwd, dim3(256), dim3(512), LDS_BYTES, stream, p);
}
```
